```python
import torch

def _softmax(x, axis=-1):
    return torch.softmax(x, dim=axis)

def _gelu(x):
    return torch.nn.functional.gelu(x, approximate='tanh')

def _silu(x):
    return torch.nn.functional.silu(x)

def _sigmoid(x):
    return torch.sigmoid(x)

def _layernorm(x, axis=-1, eps=1e-05):
    m = x.mean(dim=axis, keepdim=True)
    v = x.var(dim=axis, keepdim=True, unbiased=False)
    return (x - m) / torch.sqrt(v + eps)

def _scatter_add(tgt, idx, src, axis=0):
    return tgt.clone().index_add_(axis, idx, src)

def _segment_sum(src, seg, n):
    out = torch.zeros((n,) + src.shape[1:], dtype=src.dtype)
    return out.index_add_(0, seg, src)

def _pad(x, pw):
    flat = tuple((v for pair in reversed(pw) for v in pair))
    return torch.nn.functional.pad(x, flat)
kernel_name = 'concat_heads__memory_aug_recurrent_mem_tr__grouped_kv_mqa__prompt'

def setup_inputs():

    _w = lambda a: (lambda t: t.to(torch.bfloat16) if t.is_floating_point() else t)(torch.as_tensor(a))

    def _A_setup():
        B, S, Dm = (1, 2048, 576)
        H, Dh = (32, 128)
        M, seg = (8, 32)
        seg = min(seg, S)
        S = S // seg * seg
        return {'x': _w((lambda *s: torch.randn(*s, dtype=torch.bfloat16) * 0.05)(B, S, Dm)), 'mem0': _w((lambda *s: torch.randn(*s, dtype=torch.bfloat16) * 0.8)(B, M, Dm)), 'Wq': _w((lambda *s: torch.randn(*s, dtype=torch.bfloat16) * 1.6)(Dm, H, Dh) * Dm ** (-0.5)), 'Wk': _w((lambda *s: torch.randn(*s, dtype=torch.bfloat16) * 1.6)(Dm, H, Dh) * Dm ** (-0.5)), 'Wv': _w((lambda *s: torch.randn(*s, dtype=torch.bfloat16) * 0.8)(Dm, H, Dh) * Dm ** (-0.5)), 'Wo': _w((lambda *s: torch.randn(*s, dtype=torch.bfloat16) * 0.8)(H, Dh, Dm) * (H * Dh) ** (-0.5)), 'seg_len': seg}
    _A_in = _A_setup()

    def _B_setup():
        B, S, D, H = (1, 2048, 576, 32)
        Dh = D // H
        return {'x': _w((lambda *s: torch.randn(*s, dtype=torch.bfloat16) * 0.05)(B, S, D)), 'wq': _w((lambda *s: torch.randn(*s, dtype=torch.bfloat16) * 1.6)(D, H * Dh)), 'wk': _w((lambda *s: torch.randn(*s, dtype=torch.bfloat16) * 1.6)(D, Dh)), 'wv': _w((lambda *s: torch.randn(*s, dtype=torch.bfloat16) * 0.05)(D, Dh)), 'wo': _w((lambda *s: torch.randn(*s, dtype=torch.bfloat16) * 0.05)(H * Dh, D))}
    _B_in = _B_setup()
    return {'A_x': _A_in['x'], 'A_mem0': _A_in['mem0'], 'A_Wq': _A_in['Wq'], 'A_Wk': _A_in['Wk'], 'A_Wv': _A_in['Wv'], 'A_Wo': _A_in['Wo'], 'A_seg_len': _A_in['seg_len'], 'B_x': _B_in['x'], 'B_wq': _B_in['wq'], 'B_wk': _B_in['wk'], 'B_wv': _B_in['wv'], 'B_wo': _B_in['wo']}

def reference(A_x, A_mem0, A_Wq, A_Wk, A_Wv, A_Wo, A_seg_len, B_x, B_wq, B_wk, B_wv, B_wo):

    def _A_ref(x, mem0, Wq, Wk, Wv, Wo, seg_len):
        B, S, Dm = x.shape
        H, Dh = (Wq.shape[1], Wq.shape[2])
        M = mem0.shape[1]
        scale = Dh ** (-0.5)
        n_seg = S // seg_len
        L = M + seg_len
        idx = torch.arange(L)
        mask = (idx[:, None] < M) | (idx[:, None] >= idx[None, :])
        mem = mem0
        outs = []
        for s in range(n_seg):
            xs = x[:, s * seg_len:(s + 1) * seg_len, :].clone()
            h = torch.concatenate([mem, xs], axis=1)
            q = torch.einsum('btd,dhe->bhte', h, Wq)
            k = torch.einsum('btd,dhe->bhte', h, Wk)
            v = torch.einsum('btd,dhe->bhte', h, Wv)
            scores = torch.einsum('bhqe,bhke->bhqk', q, k) * scale
            scores = torch.where(mask[None, None], scores, -1000000000.0)
            p = _softmax(scores, axis=-1)
            o = torch.einsum('bhqk,bhke->bhqe', p, v)
            o = torch.einsum('bhte,hed->btd', o, Wo)
            mem = o[:, :M, :].clone()
            outs.append(o[:, M:, :])
        return torch.concatenate(outs, axis=1)

    def _B_ref(x, wq, wk, wv, wo):
        B, S, D = x.shape
        H = 32
        Dh = D // H
        q = (lambda x, *d: x.permute(*d))((x @ wq).reshape(B, S, H, Dh), 0, 2, 1, 3)
        k = x @ wk
        v = x @ wv
        scores = torch.einsum('bhqd,bkd->bhqk', q, k) * Dh ** (-0.5)
        p = _softmax(scores, axis=-1)
        o = torch.einsum('bhqk,bkd->bhqd', p, v)
        return (lambda x, *d: x.permute(*d))(o, 0, 2, 1, 3).reshape(B, S, H * Dh) @ wo
    a = _A_ref(A_x, A_mem0, A_Wq, A_Wk, A_Wv, A_Wo, A_seg_len)
    b = _B_ref(B_x, B_wq, B_wk, B_wv, B_wo)
    return torch.concatenate([a, b], axis=-1)
```

```cpp
#define PLACE 5
#define FULL
#include <hip/hip_runtime.h>
#include <hip/hip_bf16.h>
#include <stdint.h>

typedef __attribute__((ext_vector_type(2))) unsigned u32x2;
typedef __attribute__((ext_vector_type(4))) unsigned u32x4;
typedef __attribute__((ext_vector_type(4))) float f32x4;
typedef __attribute__((ext_vector_type(16))) float f32x16;
typedef __attribute__((ext_vector_type(8))) __bf16 bf16x8;
typedef __attribute__((ext_vector_type(4))) short s16x4;
typedef __attribute__((ext_vector_type(2))) float f32x2;
typedef __attribute__((ext_vector_type(2))) __bf16 bf16x2;
#define DEVI __device__ __forceinline__
#define LDSP __attribute__((address_space(3)))

constexpr int SEQ = 2048, DM = 576, NH = 32, DH = 128, NSEG = 64, OUTW = 1152;
constexpr unsigned FL_KV = 0;
constexpr unsigned FL_M = 32 * 256;
constexpr unsigned FL_O = 64 * 256;
constexpr unsigned FL_B = 320 * 256;
constexpr unsigned FL_INIT = 321 * 256;
constexpr unsigned FL_PAR = 322 * 256;
constexpr unsigned FL_TMO = 323 * 256;
constexpr unsigned FL_BYTES = 324 * 256;
constexpr size_t WS_WIN = 0;
constexpr size_t WS_KV = 4u << 20;
constexpr size_t WS_KM = WS_KV + (32u << 20);
constexpr size_t WS_O = WS_KM + (8u << 20);
constexpr size_t WS_BK = WS_O + (16u << 20);
constexpr size_t WS_DBG = WS_BK + (1u << 20);
constexpr unsigned SPIN_MAX = 1u << 21;

struct Params {
  const char *ax, *amem, *awq, *awk, *awv, *awo, *bx, *bwq, *bwk, *bwv, *bwo;
  char *out, *fl, *wb;
};

DEVI void wait_vm0() { asm volatile("s_waitcnt vmcnt(0)" ::: "memory"); }
template <int N> DEVI void wait_vm() { asm volatile("s_waitcnt vmcnt(%0)" ::"i"(N) : "memory"); }
DEVI void wait_lgkm0() { asm volatile("s_waitcnt lgkmcnt(0)" ::: "memory"); }
DEVI void bar() { asm volatile("s_waitcnt lgkmcnt(0)\n s_barrier" ::: "memory"); }
DEVI void glds(const char* base, unsigned voff, unsigned lds) {
  asm volatile("s_mov_b32 m0, %2\n s_nop 0\n global_load_lds_dwordx4 %0, %1" ::"v"(voff), "s"(base), "s"(lds) : "memory");
}
DEVI void glds_dev(const char* base, unsigned voff, unsigned lds) {
  asm volatile("s_mov_b32 m0, %2\n s_nop 0\n global_load_lds_dwordx4 %0, %1 sc1" ::"v"(voff), "s"(base), "s"(lds) : "memory");
}
DEVI void st16_dev(char* base, unsigned voff, u32x4 v) {
  asm volatile("global_store_dwordx4 %0, %1, %2 sc1\n s_nop 1" ::"v"(voff), "v"(v), "s"(base) : "memory");
}
DEVI void add_dev(char* p) {
  unsigned z = 0, one = 1;
  asm volatile("global_atomic_add %0, %1, %2 sc1" ::"v"(z), "v"(one), "s"(p) : "memory");
}
DEVI unsigned ld_dev(const char* p) {
  unsigned z = 0, v;
  asm volatile("global_load_dword %0, %1, %2 sc1\n s_waitcnt vmcnt(0)" : "=&v"(v) : "v"(z), "s"(p) : "memory");
  return __builtin_amdgcn_readfirstlane(v);
}
DEVI unsigned wait_ge(const char* p, unsigned want, char* tmo) {
  unsigned v = ld_dev(p);
  for (unsigned i = 0; v < want; i++) {
    if (i >= SPIN_MAX || ((i & 255) == 255 && ld_dev(tmo))) { unsigned z = 0, one = 1;
      asm volatile("global_store_dword %0, %1, %2 sc1" ::"v"(z), "v"(one), "s"(tmo) : "memory"); return 0xffffffffu; }
    __builtin_amdgcn_s_sleep(2);
    v = ld_dev(p);
  }
  return v;
}
DEVI unsigned pk(float a, float b) { f32x2 v = {a, b}; bf16x2 r = __builtin_convertvector(v, bf16x2); return __builtin_bit_cast(unsigned, r); }
DEVI u32x4 lds16(unsigned a) { return *(LDSP const u32x4*)(size_t)a; }
DEVI u32x2 ldstr(unsigned a) { return __builtin_bit_cast(u32x2, __builtin_amdgcn_ds_read_tr16_b64_v4i16((LDSP s16x4*)(size_t)a)); }
DEVI f32x16 mfma32(u32x4 a, u32x4 b, f32x16 c) {
  return __builtin_amdgcn_mfma_f32_32x32x16_bf16(__builtin_bit_cast(bf16x8, a), __builtin_bit_cast(bf16x8, b), c, 0, 0, 0);
}
DEVI u32x4 cat(u32x2 a, u32x2 b) { return u32x4{a[0], a[1], b[0], b[1]}; }

struct XTile {
  unsigned voff[9], rd[4];
  DEVI void init(int w, int l) {
#pragma unroll
    for (int i = 0; i < 9; i++) { unsigned pos = (w * 9 + i) * 64 + l, row = pos / 72, c = pos - row * 72; voff[i] = row * 1152 + ((c ^ ((row >> 1) & 7)) << 4); }
    unsigned row = l & 31, hh = l >> 5;
#pragma unroll
    for (int m = 0; m < 4; m++) rd[m] = row * 1152 + (((2 * m + hh) ^ ((row >> 1) & 7)) << 4);
  }
  DEVI void fetch(const char* x, int seg, int w, unsigned lds) const {
    const char* b = x + (size_t)seg * 32 * 1152;
#pragma unroll
    for (int i = 0; i < 9; i++) glds(b, voff[i], lds + (w * 9 + i) * 1024);
  }
  DEVI u32x4 frag(unsigned lds, int ks) const { return lds16((lds + rd[ks & 3]) + (ks >> 2) * 128); }
};

DEVI void wstage256(const char* wbase, int t, int w, int l, unsigned hoff, unsigned lds) {
  const char* b = wbase + (size_t)t * 64 * 8192;
#pragma unroll
  for (int i = 0; i < 4; i++) { unsigned row = 16 * w + 4 * i + (l >> 4); glds(b, row * 8192 + hoff + (((l & 15) ^ ((row & 3) << 2)) << 4), lds + (4 * w + i) * 1024); }
}
DEVI u32x4 wfrag256(unsigned lds, unsigned lanea, int kk) { return cat(ldstr(lds + lanea + kk * 4096), ldstr(lds + lanea + kk * 4096 + 1024)); }
DEVI unsigned wlane256(int w, int l) { unsigned q = (l >> 2) & 3, p = l & 3, g = l >> 4;
  return (8 * (g >> 1) + q) * 256 + (((4 * w + 2 * (g & 1) + (p >> 1)) ^ (q << 2)) << 4) + 8 * (p & 1); }

DEVI void role_kv(const Params& p, int h, int w, int l) {
  u32x4 wk[36], wv[36];
  const unsigned la = wlane256(w, l);
  XTile xt; xt.init(w, l);
#pragma unroll
  for (int t = 0; t < 3; t++) { wstage256(p.awk, t, w, l, h * 256, t * 32768); wstage256(p.awv, t, w, l, h * 256, t * 32768 + 16384); }
#pragma unroll
  for (int t = 0; t < 9; t++) {
    if (t < 7) wait_vm<16>(); else if (t == 7) wait_vm<8>(); else wait_vm<9>();
    bar();
    if (t + 3 < 9) { wstage256(p.awk, t + 3, w, l, h * 256, ((t + 3) & 3) * 32768); wstage256(p.awv, t + 3, w, l, h * 256, ((t + 3) & 3) * 32768 + 16384); }
    if (t == 7) xt.fetch(p.ax, 0, w, 36864);
#pragma unroll
    for (int kk = 0; kk < 4; kk++) { wk[4 * t + kk] = wfrag256((t & 3) * 32768, la, kk); wv[4 * t + kk] = wfrag256((t & 3) * 32768 + 16384, la, kk); }
#pragma unroll
    for (int kk = 0; kk < 4; kk++) { asm volatile("" : "+a"(wv[4 * t + kk])); if (t < 5) asm volatile("" : "+a"(wk[4 * t + kk])); }
  }
  char* kv = p.wb + WS_KV + (size_t)h * 16384;
  char* cnt = p.fl + FL_KV + h * 256;
  const unsigned hh = l >> 5, r = l & 31;
  const unsigned ko = (((r >> 4) * 4 + w) * 64 + hh * 16 + (r & 15)) * 16;
  const unsigned vo = 8192 + ((2 * w + (r >> 4)) * 64 + hh * 16 + (r & 15)) * 16;
#pragma unroll 1
  for (int s = 0; s < NSEG; s++) {
    unsigned xb = 36864 + (s & 1) * 36864;
    if (s == 0) wait_vm0(); else wait_vm<4>();
    bar();
    xt.fetch(p.ax, s + 1 < NSEG ? s + 1 : s, w, 36864 + ((s + 1) & 1) * 36864);
    f32x16 ak = {}, av = {};
    u32x4 xq[3] = {xt.frag(xb, 0), xt.frag(xb, 1), xt.frag(xb, 2)};
#pragma unroll
    for (int ks = 0; ks < 36; ks++) {
      const u32x4 xf = xq[ks % 3];
      if (ks + 3 < 36) xq[ks % 3] = xt.frag(xb, ks + 3);
      __builtin_amdgcn_sched_barrier(0);
      ak = mfma32(wk[ks], xf, ak);
      av = mfma32(xf, wv[ks], av);
      if (ks == 11 && s > 0) { wait_vm<9>(); if (l == 0) add_dev(cnt); }
    }
    char* o = kv + (size_t)s * 32 * 16384;
    st16_dev(o, ko, u32x4{pk(ak[0], ak[1]), pk(ak[2], ak[3]), pk(ak[4], ak[5]), pk(ak[6], ak[7])});
    st16_dev(o, ko + 512, u32x4{pk(ak[8], ak[9]), pk(ak[10], ak[11]), pk(ak[12], ak[13]), pk(ak[14], ak[15])});
    st16_dev(o, vo, u32x4{pk(av[0], av[1]), pk(av[2], av[3]), pk(av[8], av[9]), pk(av[10], av[11])});
    st16_dev(o, vo + 512, u32x4{pk(av[4], av[5]), pk(av[6], av[7]), pk(av[12], av[13]), pk(av[14], av[15])});
  }
  wait_vm0();
  if (l == 0) add_dev(cnt);
}

typedef __attribute__((ext_vector_type(8))) unsigned u32x8;
typedef __attribute__((ext_vector_type(16))) unsigned u32x16;
constexpr unsigned SENT = 0x7F817F81u;
DEVI unsigned lo32(const void* p) { return (unsigned)(size_t)p; }
DEVI unsigned hi32(const void* p) { return (unsigned)((size_t)p >> 32); }

DEVI void role_chain(const Params& p, int id, int w, int l) {
  const int h = id >> 1, a = id & 1;
  char* win = p.wb + WS_WIN;
  if (id == 0 && w == 0) {
    unsigned par = 0;
    for (int wi = 0; wi < 2; wi++) {
      unsigned best[2] = {~0u, ~0u};
      for (int rep = 0; rep < 3; rep++)
        for (int q = 0; q < 2; q++) {
          char* a0 = win + wi * (2u << 20) + 100 * 16384 + q * 8192; unsigned long long t0, t1; unsigned z = 0;
          asm volatile("s_memtime %0\n s_waitcnt lgkmcnt(0)\n global_store_dword %2, %2, %3 sc1\n s_waitcnt vmcnt(0)\n s_memtime %1\n s_waitcnt lgkmcnt(0)"
                       : "=&s"(t0), "=&s"(t1) : "v"(z), "s"(a0) : "memory");
          best[q] = min(best[q], (unsigned)(t1 - t0));
        }
      par |= (best[1] < best[0] ? 1u : 0u) << wi;
    }
    unsigned z = 0, v = par + 1;
    asm volatile("global_store_dword %0, %1, %2 sc1" ::"v"(z), "v"(v), "s"(p.fl + FL_PAR) : "memory");
  }
  const int ridx = w * 64 + id, red = ridx < 72, c = red ? ridx : 0;
  if (red) {
    const u32x4 sv = {SENT, SENT, SENT, SENT};
    for (int wi = 0; wi < 2; wi++)
      for (int j = 0; j < 16; j++) st16_dev(win + wi * (2u << 20) + c * 16384 + j * 1024, l * 16, sv);
    if (c == 0 && l < 6) st16_dev(win + 73 * 16384 + 1024, (l >> 1) * 32768 + (l & 1) * 8192, sv);
    if (l < 48) st16_dev(win + 72 * 16384 + (c < 64 ? c * 128 : 16384 + (c - 64) * 128), (l >> 4) * 32768 + ((l >> 3) & 1) * 8192 + (l & 7) * 16, sv);
  }
  const unsigned g = l >> 4, n = l & 15, q = (l >> 2) & 3, pp = l & 3, P0 = w < 2 ? 5 * w : 4 * w + 2, piece = id;
  u32x16 va, vc, vd; u32x8 vb; unsigned ve;
  va[0] = 0;
  va[1] = (l & 8) ? 0xEEEEu : 0x4444u;
  va[2] = l * 16;
  va[3] = 45056 + w * 24576 + l * 16;
  va[4] = 9216 + (32 * w + 16 * (l >> 5) + n) * 16 + 8 * (g & 1);
  va[5] = 9216 + (16 * (g >> 1) + 4 * (g & 1) + q) * 16 + 8 * (pp & 1);
  va[6] = 13312 + (2 * w + (g >> 1)) * 128 + (l & 7) * 16 + 8 * (g & 1);
  va[7] = 13312 + g * 128 + (l & 7) * 16;
  va[8] = (2 * (2 * P0 + (2 * (g & 1) + (g >> 1))) + (n >> 3)) * 16384 + piece * 128 + (l & 7) * 16;
  va[9] = (2 * (2 * (P0 + 4) + (g >> 1)) + (n >> 3)) * 16384 + piece * 128 + (l & 7) * 16 + 8 * (g & 1);
  va[10] = (c < 64 ? c * 128 : 16384 + (c - 64) * 128) + (l & 7) * 16 + (g & 1) * 8 + (l >> 5) * 4;
  va[11] = (w & 1) * 1024 + g * 128 + (l & 7) * 16;
  va[12] = 2048 + (64 * a + 16 * w + n) * 16 + 8 * (g & 1);
  va[13] = va[5] + 2048 + (w & 1) * 1024;
  va[14] = va[15] = l < 32 ? 0x3F803F80u : 0u;
#pragma unroll
  for (int i = 0; i < 4; i++) { vb[i] = 0x3F803F80u; vb[4 + i] = ((l & 7) >> 1) == i ? ((l & 1) ? 0x3F800000u : 0x3F80u) : 0u; }
#pragma unroll
  for (int i = 0; i < 4; i++) vc[i] = (16 * w + 4 * i + g) * 8192 + h * 256 + ((n ^ ((g & 1) << 1) ^ (i << 2)) << 4) - i * 1024;
#pragma unroll
  for (int i = 0; i < 2; i++) vc[4 + i] = (16 * w + 8 * i + (l >> 3)) * 8192 + h * 256 + a * 128 + (((l & 7) ^ (((2 * i + (l >> 5)) & 3) << 1)) << 4) - i * 1024;
  const unsigned trow = 16 * (g >> 1) + 4 * (g & 1) + (q & 1) + 8 * (q >> 1), phi = (q & 1) | ((g & 1) << 1) | ((q >> 1) << 2);
  vc[6] = trow * 256 + ((2 * w) ^ phi) * 32 + 8 * pp;
  vc[7] = trow * 256 + ((2 * w + 1) ^ phi) * 32 + 8 * pp;
  vc[8] = trow * 128 + (w ^ (phi >> 1)) * 32 + 8 * pp;
  const unsigned orow = 8 * (g >> 1) + 4 * (g & 1) + (q & 1);
  vc[9] = orow * 1152 + (P0 ^ (g & 1)) * 64 + (q >> 1) * 32 + 8 * pp;
  vc[10] = orow * 1152 + (((P0 + 1) ^ (g & 1)) - 1) * 64 + (q >> 1) * 32 + 8 * pp;
  vc[11] = 0;
#pragma unroll
  for (int i = 0; i < 18; i++) {
    const unsigned pos = (w * 18 + i) * 64 + l, row = pos / 72, cc = pos - row * 72, v = row * 1152 + ((cc ^ (row & 4)) << 4);
    if (i < 4) vc[12 + i] = v; else vd[i - 4] = v;
  }
  vd[14] = (l & 7) * 1152 + (l >> 3) * 16;
  vd[15] = 0;
  ve = 16384 + 8192 + w * 1024 + l * 16;
  const char* wo = p.awo + (size_t)(h * 128 + 64 * a) * 1152;
  const char* kv = p.wb + WS_KV + (size_t)h * 16384;
  const char* km = p.wb + WS_KM + (size_t)h * 4096;
  const char *s0 = p.awq, *s1 = p.awk, *s2 = p.awv, *s4 = p.amem, *s8 = p.fl;
  unsigned t0 = w, t1 = __builtin_amdgcn_readfirstlane(red), t2 = __builtin_amdgcn_readfirstlane(c), t3 = a, t4 = h, t5 = __builtin_amdgcn_readfirstlane(ridx == 0);
  asm volatile(
"s_mov_b32 s84, 0x3e0293ee\n"
"s_mov_b32 s85, 0x3e0293ee\n"
"s_mov_b32 s86, 0xbe0293ee\n"
"s_mov_b32 s92, 0x00ff00ff\n"
"s_mov_b32 s93, 0x00ff00ff\n"
"s_mov_b32 s95, 0\n"
"s_mov_b32 s2, 16384\n"
"s_lshl_b32 s3, s62, 1\n"
"s_add_u32 s3, s3, s61\n"
"s_lshl_b32 s80, s58, 11\n"
"v_add_u32 v87, s80, v2\n"
"v_add_u32 v86, 16384, v2\n"
"v_mov_b32 v85, 0x7f817f81\n"
"s_mov_b32 s88, 0\n"
"s_mov_b32 s87, 0\n"
"s_lshl_b32 s80, s62, 8\n"
"s_add_u32 s96, s56, s80\n"
"s_addc_u32 s97, s57, 0\n"
"s_add_u32 s98, s96, 8192\n"
"s_addc_u32 s99, s97, 0\n"
"s_lshl_b32 s82, s58, 12\n"
"s_add_u32 s83, s82, 0\n"
"s_mov_b32 m0, s83\n"
"s_nop 0\n"
"global_load_lds_dwordx4 v48, s[40:41]\n"
"global_load_lds_dwordx4 v49, s[40:41] offset:1024\n"
"global_load_lds_dwordx4 v50, s[40:41] offset:2048\n"
"global_load_lds_dwordx4 v51, s[40:41] offset:3072\n"
"s_add_u32 s83, s82, 16384\n"
"s_mov_b32 m0, s83\n"
"s_nop 0\n"
"global_load_lds_dwordx4 v48, s[42:43]\n"
"global_load_lds_dwordx4 v49, s[42:43] offset:1024\n"
"global_load_lds_dwordx4 v50, s[42:43] offset:2048\n"
"global_load_lds_dwordx4 v51, s[42:43] offset:3072\n"
"s_lshl_b32 s82, s58, 11\n"
"s_add_u32 s82, s82, 32768\n"
"s_mov_b32 m0, s82\n"
"s_nop 0\n"
"global_load_lds_dwordx4 v52, s[44:45]\n"
"global_load_lds_dwordx4 v53, s[44:45] offset:1024\n"
"s_add_u32 s40, s40, 524288\n"
"s_addc_u32 s41, s41, 0\n"
"s_add_u32 s42, s42, 524288\n"
"s_addc_u32 s43, s43, 0\n"
"s_add_u32 s44, s44, 524288\n"
"s_addc_u32 s45, s45, 0\n"
"s_lshl_b32 s82, s58, 12\n"
"s_add_u32 s83, s82, 40960\n"
"s_mov_b32 m0, s83\n"
"s_nop 0\n"
"global_load_lds_dwordx4 v48, s[40:41]\n"
"global_load_lds_dwordx4 v49, s[40:41] offset:1024\n"
"global_load_lds_dwordx4 v50, s[40:41] offset:2048\n"
"global_load_lds_dwordx4 v51, s[40:41] offset:3072\n"
"s_add_u32 s83, s82, 57344\n"
"s_mov_b32 m0, s83\n"
"s_nop 0\n"
"global_load_lds_dwordx4 v48, s[42:43]\n"
"global_load_lds_dwordx4 v49, s[42:43] offset:1024\n"
"global_load_lds_dwordx4 v50, s[42:43] offset:2048\n"
"global_load_lds_dwordx4 v51, s[42:43] offset:3072\n"
"s_lshl_b32 s82, s58, 11\n"
"s_add_u32 s82, s82, 73728\n"
"s_mov_b32 m0, s82\n"
"s_nop 0\n"
"global_load_lds_dwordx4 v52, s[44:45]\n"
"global_load_lds_dwordx4 v53, s[44:45] offset:1024\n"
"s_add_u32 s40, s40, 524288\n"
"s_addc_u32 s41, s41, 0\n"
"s_add_u32 s42, s42, 524288\n"
"s_addc_u32 s43, s43, 0\n"
"s_add_u32 s44, s44, 524288\n"
"s_addc_u32 s45, s45, 0\n"
"s_waitcnt vmcnt(10)\n"
"s_barrier\n"
"s_cmp_lg_u32 s58, 0\n"
"s_cbranch_scc1 CH_noinit\n"
"s_add_u32 s80, s56, 82176\n"
"s_addc_u32 s81, s57, 0\n"
"v_mov_b32 v28, 0\n"
"v_mov_b32 v29, 1\n"
"s_mov_b64 exec, 1\n"
"global_atomic_add v28, v29, s[80:81] sc1\n"
"s_mov_b64 exec, -1\n"
"s_waitcnt vmcnt(0)\n"
"CH_noinit:\n"
"s_lshl_b32 s82, s58, 12\n"
"s_add_u32 s83, s82, 81920\n"
"s_mov_b32 m0, s83\n"
"s_nop 0\n"
"global_load_lds_dwordx4 v48, s[40:41]\n"
"global_load_lds_dwordx4 v49, s[40:41] offset:1024\n"
"global_load_lds_dwordx4 v50, s[40:41] offset:2048\n"
"global_load_lds_dwordx4 v51, s[40:41] offset:3072\n"
"s_add_u32 s83, s82, 98304\n"
"s_mov_b32 m0, s83\n"
"s_nop 0\n"
"global_load_lds_dwordx4 v48, s[42:43]\n"
"global_load_lds_dwordx4 v49, s[42:43] offset:1024\n"
"global_load_lds_dwordx4 v50, s[42:43] offset:2048\n"
"global_load_lds_dwordx4 v51, s[42:43] offset:3072\n"
"s_lshl_b32 s82, s58, 11\n"
"s_add_u32 s82, s82, 114688\n"
"s_mov_b32 m0, s82\n"
"s_nop 0\n"
"global_load_lds_dwordx4 v52, s[44:45]\n"
"global_load_lds_dwordx4 v53, s[44:45] offset:1024\n"
"s_add_u32 s40, s40, 524288\n"
"s_addc_u32 s41, s41, 0\n"
"s_add_u32 s42, s42, 524288\n"
"s_addc_u32 s43, s43, 0\n"
"s_add_u32 s44, s44, 524288\n"
"s_addc_u32 s45, s45, 0\n"
"v_add_u32 v28, 0, v54\n"
"v_add_u32 v29, 0, v55\n"
"v_add_u32 v30, 32768, v56\n"
"ds_read_b64_tr_b16 v[88:89], v28 offset:0\n"
"ds_read_b64_tr_b16 a[0:1], v28 offset:16384\n"
"ds_read_b64_tr_b16 v[96:97], v29 offset:0\n"
"ds_read_b64_tr_b16 a[8:9], v29 offset:16384\n"
"ds_read_b64_tr_b16 a[144:145], v30 offset:0\n"
"ds_read_b64_tr_b16 v[90:91], v28 offset:512\n"
"ds_read_b64_tr_b16 a[2:3], v28 offset:16896\n"
"ds_read_b64_tr_b16 v[98:99], v29 offset:512\n"
"ds_read_b64_tr_b16 a[10:11], v29 offset:16896\n"
"ds_read_b64_tr_b16 a[146:147], v30 offset:256\n"
"ds_read_b64_tr_b16 v[92:93], v28 offset:8192\n"
"ds_read_b64_tr_b16 a[4:5], v28 offset:24576\n"
"ds_read_b64_tr_b16 v[100:101], v29 offset:8192\n"
"ds_read_b64_tr_b16 a[12:13], v29 offset:24576\n"
"ds_read_b64_tr_b16 a[148:149], v30 offset:4096\n"
"ds_read_b64_tr_b16 v[94:95], v28 offset:8704\n"
"ds_read_b64_tr_b16 a[6:7], v28 offset:25088\n"
"ds_read_b64_tr_b16 v[102:103], v29 offset:8704\n"
"ds_read_b64_tr_b16 a[14:15], v29 offset:25088\n"
"ds_read_b64_tr_b16 a[150:151], v30 offset:4352\n"
"s_waitcnt lgkmcnt(0)\n"
"s_waitcnt vmcnt(10)\n"
"s_barrier\n"
"s_lshl_b32 s82, s58, 12\n"
"s_add_u32 s83, s82, 0\n"
"s_mov_b32 m0, s83\n"
"s_nop 0\n"
"global_load_lds_dwordx4 v48, s[40:41]\n"
"global_load_lds_dwordx4 v49, s[40:41] offset:1024\n"
"global_load_lds_dwordx4 v50, s[40:41] offset:2048\n"
"global_load_lds_dwordx4 v51, s[40:41] offset:3072\n"
"s_add_u32 s83, s82, 16384\n"
"s_mov_b32 m0, s83\n"
"s_nop 0\n"
"global_load_lds_dwordx4 v48, s[42:43]\n"
"global_load_lds_dwordx4 v49, s[42:43] offset:1024\n"
"global_load_lds_dwordx4 v50, s[42:43] offset:2048\n"
"global_load_lds_dwordx4 v51, s[42:43] offset:3072\n"
"s_lshl_b32 s82, s58, 11\n"
"s_add_u32 s82, s82, 32768\n"
"s_mov_b32 m0, s82\n"
"s_nop 0\n"
"global_load_lds_dwordx4 v52, s[44:45]\n"
"global_load_lds_dwordx4 v53, s[44:45] offset:1024\n"
"s_add_u32 s40, s40, 524288\n"
"s_addc_u32 s41, s41, 0\n"
"s_add_u32 s42, s42, 524288\n"
"s_addc_u32 s43, s43, 0\n"
"s_add_u32 s44, s44, 524288\n"
"s_addc_u32 s45, s45, 0\n"
"v_add_u32 v28, 40960, v54\n"
"v_add_u32 v29, 40960, v55\n"
"v_add_u32 v30, 73728, v56\n"
"ds_read_b64_tr_b16 v[104:105], v28 offset:0\n"
"ds_read_b64_tr_b16 a[16:17], v28 offset:16384\n"
"ds_read_b64_tr_b16 v[112:113], v29 offset:0\n"
"ds_read_b64_tr_b16 a[24:25], v29 offset:16384\n"
"ds_read_b64_tr_b16 a[152:153], v30 offset:0\n"
"ds_read_b64_tr_b16 v[106:107], v28 offset:512\n"
"ds_read_b64_tr_b16 a[18:19], v28 offset:16896\n"
"ds_read_b64_tr_b16 v[114:115], v29 offset:512\n"
"ds_read_b64_tr_b16 a[26:27], v29 offset:16896\n"
"ds_read_b64_tr_b16 a[154:155], v30 offset:256\n"
"ds_read_b64_tr_b16 v[108:109], v28 offset:8192\n"
"ds_read_b64_tr_b16 a[20:21], v28 offset:24576\n"
"ds_read_b64_tr_b16 v[116:117], v29 offset:8192\n"
"ds_read_b64_tr_b16 a[28:29], v29 offset:24576\n"
"ds_read_b64_tr_b16 a[156:157], v30 offset:4096\n"
"ds_read_b64_tr_b16 v[110:111], v28 offset:8704\n"
"ds_read_b64_tr_b16 a[22:23], v28 offset:25088\n"
"ds_read_b64_tr_b16 v[118:119], v29 offset:8704\n"
"ds_read_b64_tr_b16 a[30:31], v29 offset:25088\n"
"ds_read_b64_tr_b16 a[158:159], v30 offset:4352\n"
"s_waitcnt lgkmcnt(0)\n"
"s_waitcnt vmcnt(10)\n"
"s_barrier\n"
"s_lshl_b32 s82, s58, 12\n"
"s_add_u32 s83, s82, 40960\n"
"s_mov_b32 m0, s83\n"
"s_nop 0\n"
"global_load_lds_dwordx4 v48, s[40:41]\n"
"global_load_lds_dwordx4 v49, s[40:41] offset:1024\n"
"global_load_lds_dwordx4 v50, s[40:41] offset:2048\n"
"global_load_lds_dwordx4 v51, s[40:41] offset:3072\n"
"s_add_u32 s83, s82, 57344\n"
"s_mov_b32 m0, s83\n"
"s_nop 0\n"
"global_load_lds_dwordx4 v48, s[42:43]\n"
"global_load_lds_dwordx4 v49, s[42:43] offset:1024\n"
"global_load_lds_dwordx4 v50, s[42:43] offset:2048\n"
"global_load_lds_dwordx4 v51, s[42:43] offset:3072\n"
"s_lshl_b32 s82, s58, 11\n"
"s_add_u32 s82, s82, 73728\n"
"s_mov_b32 m0, s82\n"
"s_nop 0\n"
"global_load_lds_dwordx4 v52, s[44:45]\n"
"global_load_lds_dwordx4 v53, s[44:45] offset:1024\n"
"s_add_u32 s40, s40, 524288\n"
"s_addc_u32 s41, s41, 0\n"
"s_add_u32 s42, s42, 524288\n"
"s_addc_u32 s43, s43, 0\n"
"s_add_u32 s44, s44, 524288\n"
"s_addc_u32 s45, s45, 0\n"
"v_add_u32 v28, 81920, v54\n"
"v_add_u32 v29, 81920, v55\n"
"v_add_u32 v30, 114688, v56\n"
"ds_read_b64_tr_b16 v[120:121], v28 offset:0\n"
"ds_read_b64_tr_b16 a[32:33], v28 offset:16384\n"
"ds_read_b64_tr_b16 v[128:129], v29 offset:0\n"
"ds_read_b64_tr_b16 a[40:41], v29 offset:16384\n"
"ds_read_b64_tr_b16 a[160:161], v30 offset:0\n"
"ds_read_b64_tr_b16 v[122:123], v28 offset:512\n"
"ds_read_b64_tr_b16 a[34:35], v28 offset:16896\n"
"ds_read_b64_tr_b16 v[130:131], v29 offset:512\n"
"ds_read_b64_tr_b16 a[42:43], v29 offset:16896\n"
"ds_read_b64_tr_b16 a[162:163], v30 offset:256\n"
"ds_read_b64_tr_b16 v[124:125], v28 offset:8192\n"
"ds_read_b64_tr_b16 a[36:37], v28 offset:24576\n"
"ds_read_b64_tr_b16 v[132:133], v29 offset:8192\n"
"ds_read_b64_tr_b16 a[44:45], v29 offset:24576\n"
"ds_read_b64_tr_b16 a[164:165], v30 offset:4096\n"
"ds_read_b64_tr_b16 v[126:127], v28 offset:8704\n"
"ds_read_b64_tr_b16 a[38:39], v28 offset:25088\n"
"ds_read_b64_tr_b16 v[134:135], v29 offset:8704\n"
"ds_read_b64_tr_b16 a[46:47], v29 offset:25088\n"
"ds_read_b64_tr_b16 a[166:167], v30 offset:4352\n"
"s_waitcnt lgkmcnt(0)\n"
"s_waitcnt vmcnt(10)\n"
"s_barrier\n"
"s_lshl_b32 s82, s58, 12\n"
"s_add_u32 s83, s82, 81920\n"
"s_mov_b32 m0, s83\n"
"s_nop 0\n"
"global_load_lds_dwordx4 v48, s[40:41]\n"
"global_load_lds_dwordx4 v49, s[40:41] offset:1024\n"
"global_load_lds_dwordx4 v50, s[40:41] offset:2048\n"
"global_load_lds_dwordx4 v51, s[40:41] offset:3072\n"
"s_add_u32 s83, s82, 98304\n"
"s_mov_b32 m0, s83\n"
"s_nop 0\n"
"global_load_lds_dwordx4 v48, s[42:43]\n"
"global_load_lds_dwordx4 v49, s[42:43] offset:1024\n"
"global_load_lds_dwordx4 v50, s[42:43] offset:2048\n"
"global_load_lds_dwordx4 v51, s[42:43] offset:3072\n"
"s_lshl_b32 s82, s58, 11\n"
"s_add_u32 s82, s82, 114688\n"
"s_mov_b32 m0, s82\n"
"s_nop 0\n"
"global_load_lds_dwordx4 v52, s[44:45]\n"
"global_load_lds_dwordx4 v53, s[44:45] offset:1024\n"
"s_add_u32 s40, s40, 524288\n"
"s_addc_u32 s41, s41, 0\n"
"s_add_u32 s42, s42, 524288\n"
"s_addc_u32 s43, s43, 0\n"
"s_add_u32 s44, s44, 524288\n"
"s_addc_u32 s45, s45, 0\n"
"v_add_u32 v28, 0, v54\n"
"v_add_u32 v29, 0, v55\n"
"v_add_u32 v30, 32768, v56\n"
"ds_read_b64_tr_b16 v[136:137], v28 offset:0\n"
"ds_read_b64_tr_b16 a[48:49], v28 offset:16384\n"
"ds_read_b64_tr_b16 v[144:145], v29 offset:0\n"
"ds_read_b64_tr_b16 a[56:57], v29 offset:16384\n"
"ds_read_b64_tr_b16 a[168:169], v30 offset:0\n"
"ds_read_b64_tr_b16 v[138:139], v28 offset:512\n"
"ds_read_b64_tr_b16 a[50:51], v28 offset:16896\n"
"ds_read_b64_tr_b16 v[146:147], v29 offset:512\n"
"ds_read_b64_tr_b16 a[58:59], v29 offset:16896\n"
"ds_read_b64_tr_b16 a[170:171], v30 offset:256\n"
"ds_read_b64_tr_b16 v[140:141], v28 offset:8192\n"
"ds_read_b64_tr_b16 a[52:53], v28 offset:24576\n"
"ds_read_b64_tr_b16 v[148:149], v29 offset:8192\n"
"ds_read_b64_tr_b16 a[60:61], v29 offset:24576\n"
"ds_read_b64_tr_b16 a[172:173], v30 offset:4096\n"
"ds_read_b64_tr_b16 v[142:143], v28 offset:8704\n"
"ds_read_b64_tr_b16 a[54:55], v28 offset:25088\n"
"ds_read_b64_tr_b16 v[150:151], v29 offset:8704\n"
"ds_read_b64_tr_b16 a[62:63], v29 offset:25088\n"
"ds_read_b64_tr_b16 a[174:175], v30 offset:4352\n"
"s_waitcnt lgkmcnt(0)\n"
"s_waitcnt vmcnt(10)\n"
"s_barrier\n"
"s_lshl_b32 s82, s58, 12\n"
"s_add_u32 s83, s82, 0\n"
"s_mov_b32 m0, s83\n"
"s_nop 0\n"
"global_load_lds_dwordx4 v48, s[40:41]\n"
"global_load_lds_dwordx4 v49, s[40:41] offset:1024\n"
"global_load_lds_dwordx4 v50, s[40:41] offset:2048\n"
"global_load_lds_dwordx4 v51, s[40:41] offset:3072\n"
"s_add_u32 s83, s82, 16384\n"
"s_mov_b32 m0, s83\n"
"s_nop 0\n"
"global_load_lds_dwordx4 v48, s[42:43]\n"
"global_load_lds_dwordx4 v49, s[42:43] offset:1024\n"
"global_load_lds_dwordx4 v50, s[42:43] offset:2048\n"
"global_load_lds_dwordx4 v51, s[42:43] offset:3072\n"
"s_lshl_b32 s82, s58, 11\n"
"s_add_u32 s82, s82, 32768\n"
"s_mov_b32 m0, s82\n"
"s_nop 0\n"
"global_load_lds_dwordx4 v52, s[44:45]\n"
"global_load_lds_dwordx4 v53, s[44:45] offset:1024\n"
"s_add_u32 s40, s40, 524288\n"
"s_addc_u32 s41, s41, 0\n"
"s_add_u32 s42, s42, 524288\n"
"s_addc_u32 s43, s43, 0\n"
"s_add_u32 s44, s44, 524288\n"
"s_addc_u32 s45, s45, 0\n"
"v_add_u32 v28, 40960, v54\n"
"v_add_u32 v29, 40960, v55\n"
"v_add_u32 v30, 73728, v56\n"
"ds_read_b64_tr_b16 v[152:153], v28 offset:0\n"
"ds_read_b64_tr_b16 a[64:65], v28 offset:16384\n"
"ds_read_b64_tr_b16 v[160:161], v29 offset:0\n"
"ds_read_b64_tr_b16 a[72:73], v29 offset:16384\n"
"ds_read_b64_tr_b16 a[176:177], v30 offset:0\n"
"ds_read_b64_tr_b16 v[154:155], v28 offset:512\n"
"ds_read_b64_tr_b16 a[66:67], v28 offset:16896\n"
"ds_read_b64_tr_b16 v[162:163], v29 offset:512\n"
"ds_read_b64_tr_b16 a[74:75], v29 offset:16896\n"
"ds_read_b64_tr_b16 a[178:179], v30 offset:256\n"
"ds_read_b64_tr_b16 v[156:157], v28 offset:8192\n"
"ds_read_b64_tr_b16 a[68:69], v28 offset:24576\n"
"ds_read_b64_tr_b16 v[164:165], v29 offset:8192\n"
"ds_read_b64_tr_b16 a[76:77], v29 offset:24576\n"
"ds_read_b64_tr_b16 a[180:181], v30 offset:4096\n"
"ds_read_b64_tr_b16 v[158:159], v28 offset:8704\n"
"ds_read_b64_tr_b16 a[70:71], v28 offset:25088\n"
"ds_read_b64_tr_b16 v[166:167], v29 offset:8704\n"
"ds_read_b64_tr_b16 a[78:79], v29 offset:25088\n"
"ds_read_b64_tr_b16 a[182:183], v30 offset:4352\n"
"s_waitcnt lgkmcnt(0)\n"
"s_waitcnt vmcnt(10)\n"
"s_barrier\n"
"s_lshl_b32 s82, s58, 12\n"
"s_add_u32 s83, s82, 40960\n"
"s_mov_b32 m0, s83\n"
"s_nop 0\n"
"global_load_lds_dwordx4 v48, s[40:41]\n"
"global_load_lds_dwordx4 v49, s[40:41] offset:1024\n"
"global_load_lds_dwordx4 v50, s[40:41] offset:2048\n"
"global_load_lds_dwordx4 v51, s[40:41] offset:3072\n"
"s_add_u32 s83, s82, 57344\n"
"s_mov_b32 m0, s83\n"
"s_nop 0\n"
"global_load_lds_dwordx4 v48, s[42:43]\n"
"global_load_lds_dwordx4 v49, s[42:43] offset:1024\n"
"global_load_lds_dwordx4 v50, s[42:43] offset:2048\n"
"global_load_lds_dwordx4 v51, s[42:43] offset:3072\n"
"s_lshl_b32 s82, s58, 11\n"
"s_add_u32 s82, s82, 73728\n"
"s_mov_b32 m0, s82\n"
"s_nop 0\n"
"global_load_lds_dwordx4 v52, s[44:45]\n"
"global_load_lds_dwordx4 v53, s[44:45] offset:1024\n"
"s_add_u32 s40, s40, 524288\n"
"s_addc_u32 s41, s41, 0\n"
"s_add_u32 s42, s42, 524288\n"
"s_addc_u32 s43, s43, 0\n"
"s_add_u32 s44, s44, 524288\n"
"s_addc_u32 s45, s45, 0\n"
"v_add_u32 v28, 81920, v54\n"
"v_add_u32 v29, 81920, v55\n"
"v_add_u32 v30, 114688, v56\n"
"ds_read_b64_tr_b16 v[168:169], v28 offset:0\n"
"ds_read_b64_tr_b16 a[80:81], v28 offset:16384\n"
"ds_read_b64_tr_b16 v[176:177], v29 offset:0\n"
"ds_read_b64_tr_b16 a[88:89], v29 offset:16384\n"
"ds_read_b64_tr_b16 a[184:185], v30 offset:0\n"
"ds_read_b64_tr_b16 v[170:171], v28 offset:512\n"
"ds_read_b64_tr_b16 a[82:83], v28 offset:16896\n"
"ds_read_b64_tr_b16 v[178:179], v29 offset:512\n"
"ds_read_b64_tr_b16 a[90:91], v29 offset:16896\n"
"ds_read_b64_tr_b16 a[186:187], v30 offset:256\n"
"ds_read_b64_tr_b16 v[172:173], v28 offset:8192\n"
"ds_read_b64_tr_b16 a[84:85], v28 offset:24576\n"
"ds_read_b64_tr_b16 v[180:181], v29 offset:8192\n"
"ds_read_b64_tr_b16 a[92:93], v29 offset:24576\n"
"ds_read_b64_tr_b16 a[188:189], v30 offset:4096\n"
"ds_read_b64_tr_b16 v[174:175], v28 offset:8704\n"
"ds_read_b64_tr_b16 a[86:87], v28 offset:25088\n"
"ds_read_b64_tr_b16 v[182:183], v29 offset:8704\n"
"ds_read_b64_tr_b16 a[94:95], v29 offset:25088\n"
"ds_read_b64_tr_b16 a[190:191], v30 offset:4352\n"
"s_waitcnt lgkmcnt(0)\n"
"s_waitcnt vmcnt(10)\n"
"s_barrier\n"
"s_lshl_b32 s82, s58, 12\n"
"s_add_u32 s83, s82, 81920\n"
"s_mov_b32 m0, s83\n"
"s_nop 0\n"
"global_load_lds_dwordx4 v48, s[40:41]\n"
"global_load_lds_dwordx4 v49, s[40:41] offset:1024\n"
"global_load_lds_dwordx4 v50, s[40:41] offset:2048\n"
"global_load_lds_dwordx4 v51, s[40:41] offset:3072\n"
"s_add_u32 s83, s82, 98304\n"
"s_mov_b32 m0, s83\n"
"s_nop 0\n"
"global_load_lds_dwordx4 v48, s[42:43]\n"
"global_load_lds_dwordx4 v49, s[42:43] offset:1024\n"
"global_load_lds_dwordx4 v50, s[42:43] offset:2048\n"
"global_load_lds_dwordx4 v51, s[42:43] offset:3072\n"
"s_lshl_b32 s82, s58, 11\n"
"s_add_u32 s82, s82, 114688\n"
"s_mov_b32 m0, s82\n"
"s_nop 0\n"
"global_load_lds_dwordx4 v52, s[44:45]\n"
"global_load_lds_dwordx4 v53, s[44:45] offset:1024\n"
"s_add_u32 s40, s40, 524288\n"
"s_addc_u32 s41, s41, 0\n"
"s_add_u32 s42, s42, 524288\n"
"s_addc_u32 s43, s43, 0\n"
"s_add_u32 s44, s44, 524288\n"
"s_addc_u32 s45, s45, 0\n"
"v_add_u32 v28, 0, v54\n"
"v_add_u32 v29, 0, v55\n"
"v_add_u32 v30, 32768, v56\n"
"ds_read_b64_tr_b16 v[184:185], v28 offset:0\n"
"ds_read_b64_tr_b16 a[96:97], v28 offset:16384\n"
"ds_read_b64_tr_b16 v[192:193], v29 offset:0\n"
"ds_read_b64_tr_b16 a[104:105], v29 offset:16384\n"
"ds_read_b64_tr_b16 a[192:193], v30 offset:0\n"
"ds_read_b64_tr_b16 v[186:187], v28 offset:512\n"
"ds_read_b64_tr_b16 a[98:99], v28 offset:16896\n"
"ds_read_b64_tr_b16 v[194:195], v29 offset:512\n"
"ds_read_b64_tr_b16 a[106:107], v29 offset:16896\n"
"ds_read_b64_tr_b16 a[194:195], v30 offset:256\n"
"ds_read_b64_tr_b16 v[188:189], v28 offset:8192\n"
"ds_read_b64_tr_b16 a[100:101], v28 offset:24576\n"
"ds_read_b64_tr_b16 v[196:197], v29 offset:8192\n"
"ds_read_b64_tr_b16 a[108:109], v29 offset:24576\n"
"ds_read_b64_tr_b16 a[196:197], v30 offset:4096\n"
"ds_read_b64_tr_b16 v[190:191], v28 offset:8704\n"
"ds_read_b64_tr_b16 a[102:103], v28 offset:25088\n"
"ds_read_b64_tr_b16 v[198:199], v29 offset:8704\n"
"ds_read_b64_tr_b16 a[110:111], v29 offset:25088\n"
"ds_read_b64_tr_b16 a[198:199], v30 offset:4352\n"
"s_waitcnt lgkmcnt(0)\n"
"s_waitcnt vmcnt(10)\n"
"s_barrier\n"
"v_add_u32 v28, 40960, v54\n"
"v_add_u32 v29, 40960, v55\n"
"v_add_u32 v30, 73728, v56\n"
"ds_read_b64_tr_b16 v[200:201], v28 offset:0\n"
"ds_read_b64_tr_b16 a[112:113], v28 offset:16384\n"
"ds_read_b64_tr_b16 v[208:209], v29 offset:0\n"
"ds_read_b64_tr_b16 a[120:121], v29 offset:16384\n"
"ds_read_b64_tr_b16 a[200:201], v30 offset:0\n"
"ds_read_b64_tr_b16 v[202:203], v28 offset:512\n"
"ds_read_b64_tr_b16 a[114:115], v28 offset:16896\n"
"ds_read_b64_tr_b16 v[210:211], v29 offset:512\n"
"ds_read_b64_tr_b16 a[122:123], v29 offset:16896\n"
"ds_read_b64_tr_b16 a[202:203], v30 offset:256\n"
"ds_read_b64_tr_b16 v[204:205], v28 offset:8192\n"
"ds_read_b64_tr_b16 a[116:117], v28 offset:24576\n"
"ds_read_b64_tr_b16 v[212:213], v29 offset:8192\n"
"ds_read_b64_tr_b16 a[124:125], v29 offset:24576\n"
"ds_read_b64_tr_b16 a[204:205], v30 offset:4096\n"
"ds_read_b64_tr_b16 v[206:207], v28 offset:8704\n"
"ds_read_b64_tr_b16 a[118:119], v28 offset:25088\n"
"ds_read_b64_tr_b16 v[214:215], v29 offset:8704\n"
"ds_read_b64_tr_b16 a[126:127], v29 offset:25088\n"
"ds_read_b64_tr_b16 a[206:207], v30 offset:4352\n"
"s_waitcnt lgkmcnt(0)\n"
"s_waitcnt vmcnt(0)\n"
"s_barrier\n"
"v_add_u32 v28, 81920, v54\n"
"v_add_u32 v29, 81920, v55\n"
"v_add_u32 v30, 114688, v56\n"
"ds_read_b64_tr_b16 v[216:217], v28 offset:0\n"
"ds_read_b64_tr_b16 a[128:129], v28 offset:16384\n"
"ds_read_b64_tr_b16 v[224:225], v29 offset:0\n"
"ds_read_b64_tr_b16 a[136:137], v29 offset:16384\n"
"ds_read_b64_tr_b16 a[208:209], v30 offset:0\n"
"ds_read_b64_tr_b16 v[218:219], v28 offset:512\n"
"ds_read_b64_tr_b16 a[130:131], v28 offset:16896\n"
"ds_read_b64_tr_b16 v[226:227], v29 offset:512\n"
"ds_read_b64_tr_b16 a[138:139], v29 offset:16896\n"
"ds_read_b64_tr_b16 a[210:211], v30 offset:256\n"
"ds_read_b64_tr_b16 v[220:221], v28 offset:8192\n"
"ds_read_b64_tr_b16 a[132:133], v28 offset:24576\n"
"ds_read_b64_tr_b16 v[228:229], v29 offset:8192\n"
"ds_read_b64_tr_b16 a[140:141], v29 offset:24576\n"
"ds_read_b64_tr_b16 a[212:213], v30 offset:4096\n"
"ds_read_b64_tr_b16 v[222:223], v28 offset:8704\n"
"ds_read_b64_tr_b16 a[134:135], v28 offset:25088\n"
"ds_read_b64_tr_b16 v[230:231], v29 offset:8704\n"
"ds_read_b64_tr_b16 a[142:143], v29 offset:25088\n"
"ds_read_b64_tr_b16 a[214:215], v30 offset:4352\n"
"s_waitcnt lgkmcnt(0)\n"
"s_barrier\n"
"s_mul_i32 s82, s58, 18432\n"
"s_add_u32 s83, s82, 0\n"
"s_mov_b32 m0, s83\n"
"s_nop 0\n"
"global_load_lds_dwordx4 v60, s[46:47]\n"
"s_add_u32 s83, s82, 1024\n"
"s_mov_b32 m0, s83\n"
"s_nop 0\n"
"global_load_lds_dwordx4 v61, s[46:47]\n"
"s_add_u32 s83, s82, 2048\n"
"s_mov_b32 m0, s83\n"
"s_nop 0\n"
"global_load_lds_dwordx4 v62, s[46:47]\n"
"s_add_u32 s83, s82, 3072\n"
"s_mov_b32 m0, s83\n"
"s_nop 0\n"
"global_load_lds_dwordx4 v63, s[46:47]\n"
"s_add_u32 s83, s82, 4096\n"
"s_mov_b32 m0, s83\n"
"s_nop 0\n"
"global_load_lds_dwordx4 v64, s[46:47]\n"
"s_add_u32 s83, s82, 5120\n"
"s_mov_b32 m0, s83\n"
"s_nop 0\n"
"global_load_lds_dwordx4 v65, s[46:47]\n"
"s_add_u32 s83, s82, 6144\n"
"s_mov_b32 m0, s83\n"
"s_nop 0\n"
"global_load_lds_dwordx4 v66, s[46:47]\n"
"s_add_u32 s83, s82, 7168\n"
"s_mov_b32 m0, s83\n"
"s_nop 0\n"
"global_load_lds_dwordx4 v67, s[46:47]\n"
"s_add_u32 s83, s82, 8192\n"
"s_mov_b32 m0, s83\n"
"s_nop 0\n"
"global_load_lds_dwordx4 v68, s[46:47]\n"
"s_add_u32 s83, s82, 9216\n"
"s_mov_b32 m0, s83\n"
"s_nop 0\n"
"global_load_lds_dwordx4 v69, s[46:47]\n"
"s_add_u32 s83, s82, 10240\n"
"s_mov_b32 m0, s83\n"
"s_nop 0\n"
"global_load_lds_dwordx4 v70, s[46:47]\n"
"s_add_u32 s83, s82, 11264\n"
"s_mov_b32 m0, s83\n"
"s_nop 0\n"
"global_load_lds_dwordx4 v71, s[46:47]\n"
"s_add_u32 s83, s82, 12288\n"
"s_mov_b32 m0, s83\n"
"s_nop 0\n"
"global_load_lds_dwordx4 v72, s[46:47]\n"
"s_add_u32 s83, s82, 13312\n"
"s_mov_b32 m0, s83\n"
"s_nop 0\n"
"global_load_lds_dwordx4 v73, s[46:47]\n"
"s_add_u32 s83, s82, 14336\n"
"s_mov_b32 m0, s83\n"
"s_nop 0\n"
"global_load_lds_dwordx4 v74, s[46:47]\n"
"s_add_u32 s83, s82, 15360\n"
"s_mov_b32 m0, s83\n"
"s_nop 0\n"
"global_load_lds_dwordx4 v75, s[46:47]\n"
"s_add_u32 s83, s82, 16384\n"
"s_mov_b32 m0, s83\n"
"s_nop 0\n"
"global_load_lds_dwordx4 v76, s[46:47]\n"
"s_add_u32 s83, s82, 17408\n"
"s_mov_b32 m0, s83\n"
"s_nop 0\n"
"global_load_lds_dwordx4 v77, s[46:47]\n"
"s_add_u32 s80, s56, 82432\n"
"s_addc_u32 s81, s57, 0\n"
"s_mov_b32 s89, 0\n"
"v_mov_b32 v28, 0\n"
"CH_par_p:\n"
"global_load_dword v29, v28, s[80:81] sc1\n"
"s_waitcnt vmcnt(0)\n"
"v_readfirstlane_b32 s82, v29\n"
"s_nop 3\n"
"s_cmp_ge_u32 s82, 1\n"
"s_cbranch_scc1 CH_par_d\n"
"s_add_u32 s89, s89, 1\n"
"s_cmp_lt_u32 s89, 0x100000\n"
"s_sleep 1\n"
"s_cbranch_scc1 CH_par_p\n"
"s_branch CH_abort\n"
"CH_par_d:\n"
"s_sub_u32 s82, s82, 1\n"
"s_and_b32 s80, s82, 1\n"
"s_lshl_b32 s80, s80, 13\n"
"s_add_u32 s70, s50, s80\n"
"s_addc_u32 s71, s51, 0\n"
"s_and_b32 s80, s82, 2\n"
"s_lshl_b32 s80, s80, 12\n"
"s_add_u32 s80, s80, 0x200000\n"
"s_add_u32 s72, s50, s80\n"
"s_addc_u32 s73, s51, 0\n"
"s_add_u32 s74, s70, 1179648\n"
"s_addc_u32 s75, s71, 0\n"
"s_add_u32 s76, s70, 1212416\n"
"s_addc_u32 s77, s71, 0\n"
"s_add_u32 s78, s70, 1245184\n"
"s_addc_u32 s79, s71, 0\n"
"s_barrier\n"
"ds_read_b64_tr_b16 a[216:217], v57 offset:0\n"
"ds_read_b64_tr_b16 a[218:219], v57 offset:2304\n"
"ds_read_b64_tr_b16 a[220:221], v57 offset:18432\n"
"ds_read_b64_tr_b16 a[222:223], v57 offset:20736\n"
"ds_read_b64_tr_b16 a[224:225], v57 offset:36864\n"
"ds_read_b64_tr_b16 a[226:227], v57 offset:39168\n"
"ds_read_b64_tr_b16 a[228:229], v57 offset:55296\n"
"ds_read_b64_tr_b16 a[230:231], v57 offset:57600\n"
"ds_read_b64_tr_b16 a[232:233], v58 offset:64\n"
"ds_read_b64_tr_b16 a[234:235], v58 offset:2368\n"
"ds_read_b64_tr_b16 a[236:237], v58 offset:18496\n"
"ds_read_b64_tr_b16 a[238:239], v58 offset:20800\n"
"ds_read_b64_tr_b16 a[240:241], v58 offset:36928\n"
"ds_read_b64_tr_b16 a[242:243], v58 offset:39232\n"
"ds_read_b64_tr_b16 a[244:245], v58 offset:55360\n"
"ds_read_b64_tr_b16 a[246:247], v58 offset:57664\n"
"ds_read_b64_tr_b16 a[248:249], v57 offset:128\n"
"ds_read_b64_tr_b16 a[250:251], v57 offset:2432\n"
"ds_read_b64_tr_b16 a[252:253], v57 offset:18560\n"
"ds_read_b64_tr_b16 a[254:255], v57 offset:20864\n"
"ds_read_b64_tr_b16 v[232:233], v57 offset:36992\n"
"ds_read_b64_tr_b16 v[234:235], v57 offset:39296\n"
"ds_read_b64_tr_b16 v[236:237], v57 offset:55424\n"
"ds_read_b64_tr_b16 v[238:239], v57 offset:57728\n"
"ds_read_b64_tr_b16 v[240:241], v58 offset:192\n"
"ds_read_b64_tr_b16 v[242:243], v58 offset:2496\n"
"ds_read_b64_tr_b16 v[244:245], v58 offset:18624\n"
"ds_read_b64_tr_b16 v[246:247], v58 offset:20928\n"
"ds_read_b64_tr_b16 v[248:249], v58 offset:37056\n"
"ds_read_b64_tr_b16 v[250:251], v58 offset:39360\n"
"ds_read_b64_tr_b16 v[252:253], v58 offset:55488\n"
"ds_read_b64_tr_b16 v[254:255], v58 offset:57792\n"
"ds_read_b64_tr_b16 v[60:61], v57 offset:256\n"
"ds_read_b64_tr_b16 v[62:63], v57 offset:2560\n"
"ds_read_b64_tr_b16 v[64:65], v57 offset:18688\n"
"ds_read_b64_tr_b16 v[66:67], v57 offset:20992\n"
"ds_read_b64_tr_b16 v[68:69], v57 offset:37120\n"
"ds_read_b64_tr_b16 v[70:71], v57 offset:39424\n"
"ds_read_b64_tr_b16 v[72:73], v57 offset:55552\n"
"ds_read_b64_tr_b16 v[74:75], v57 offset:57856\n"
"s_waitcnt lgkmcnt(0)\n"
"s_barrier\n"
"ds_write_b128 v3, v[88:91] offset:0\n"
"ds_write_b128 v3, v[92:95] offset:1024\n"
"ds_write_b128 v3, v[96:99] offset:2048\n"
"ds_write_b128 v3, v[100:103] offset:3072\n"
"ds_write_b128 v3, v[104:107] offset:4096\n"
"ds_write_b128 v3, v[108:111] offset:5120\n"
"ds_write_b128 v3, v[112:115] offset:6144\n"
"ds_write_b128 v3, v[116:119] offset:7168\n"
"ds_write_b128 v3, v[120:123] offset:8192\n"
"ds_write_b128 v3, v[124:127] offset:9216\n"
"ds_write_b128 v3, v[128:131] offset:10240\n"
"ds_write_b128 v3, v[132:135] offset:11264\n"
"ds_write_b128 v3, v[136:139] offset:12288\n"
"ds_write_b128 v3, v[140:143] offset:13312\n"
"ds_write_b128 v3, v[144:147] offset:14336\n"
"ds_write_b128 v3, v[148:151] offset:15360\n"
"ds_write_b128 v3, v[152:155] offset:16384\n"
"ds_write_b128 v3, v[156:159] offset:17408\n"
"ds_write_b128 v3, v[160:163] offset:18432\n"
"ds_write_b128 v3, v[164:167] offset:19456\n"
"ds_write_b128 v3, v[60:63] offset:20480\n"
"ds_write_b128 v3, v[64:67] offset:21504\n"
"ds_write_b128 v3, v[68:71] offset:22528\n"
"ds_write_b128 v3, v[72:75] offset:23552\n"
"s_lshl_b32 s82, s58, 8\n"
"s_add_u32 s80, s48, s82\n"
"s_addc_u32 s81, s49, 0\n"
"s_lshl_b32 s82, s58, 11\n"
"s_mov_b32 m0, s82\n"
"s_nop 0\n"
"global_load_lds_dwordx4 v78, s[80:81]\n"
"s_add_u32 s80, s80, 128\n"
"s_addc_u32 s81, s81, 0\n"
"s_add_u32 s82, s82, 1024\n"
"s_mov_b32 m0, s82\n"
"s_nop 0\n"
"global_load_lds_dwordx4 v78, s[80:81]\n"
"s_cmp_lg_u32 s58, 3\n"
"s_cbranch_scc1 CH_m0d\n"
"s_add_u32 s80, s48, 1024\n"
"s_addc_u32 s81, s49, 0\n"
"s_mov_b32 m0, 8192\n"
"s_nop 0\n"
"global_load_lds_dwordx4 v78, s[80:81]\n"
"CH_m0d:\n"
"s_cmp_lt_u32 s58, 2\n"
"s_cbranch_scc1 CH_kv0d\n"
"s_mov_b32 s89, 0\n"
"v_mov_b32 v28, 0\n"
"CH_kv0_p:\n"
"global_load_dword v29, v28, s[96:97] sc1\n"
"s_waitcnt vmcnt(0)\n"
"v_readfirstlane_b32 s88, v29\n"
"s_nop 3\n"
"s_cmp_ge_u32 s88, 4\n"
"s_cbranch_scc1 CH_kv0_d\n"
"s_add_u32 s89, s89, 1\n"
"s_cmp_lt_u32 s89, 0x100000\n"
"s_sleep 1\n"
"s_cbranch_scc1 CH_kv0_p\n"
"s_branch CH_abort\n"
"CH_kv0_d:\n"
"s_sub_u32 s82, s58, 2\n"
"s_lshl_b32 s82, s82, 12\n"
"s_add_u32 s80, s52, s82\n"
"s_addc_u32 s81, s53, 0\n"
"s_add_u32 s82, s82, s2\n"
"s_mov_b32 m0, s82\n"
"s_nop 0\n"
"global_load_lds_dwordx4 v2, s[80:81] sc1\n"
"global_load_lds_dwordx4 v2, s[80:81] offset:1024 sc1\n"
"global_load_lds_dwordx4 v2, s[80:81] offset:2048 sc1\n"
"global_load_lds_dwordx4 v2, s[80:81] offset:3072 sc1\n"
"s_sub_u32 s82, s58, 2\n"
"s_lshl_b32 s82, s82, 11\n"
"s_add_u32 s82, s82, 8192\n"
"s_add_u32 s83, s82, s2\n"
"s_mov_b32 m0, s83\n"
"s_lshl_b32 s83, s61, 12\n"
"s_add_u32 s82, s82, s83\n"
"s_add_u32 s80, s52, s82\n"
"s_addc_u32 s81, s53, 0\n"
"global_load_lds_dwordx4 v2, s[80:81] sc1\n"
"global_load_lds_dwordx4 v2, s[80:81] offset:1024 sc1\n"
"s_add_u32 s52, s52, 524288\n"
"s_addc_u32 s53, s53, 0\n"
"s_xor_b32 s2, s2, 49152\n"
"CH_kv0d:\n"
"s_add_u32 s80, s56, 82176\n"
"s_addc_u32 s81, s57, 0\n"
"s_mov_b32 s89, 0\n"
"v_mov_b32 v28, 0\n"
"CH_init_p:\n"
"global_load_dword v29, v28, s[80:81] sc1\n"
"s_waitcnt vmcnt(0)\n"
"v_readfirstlane_b32 s82, v29\n"
"s_nop 3\n"
"s_cmp_ge_u32 s82, 64\n"
"s_cbranch_scc1 CH_init_d\n"
"s_add_u32 s89, s89, 1\n"
"s_cmp_lt_u32 s89, 0x100000\n"
"s_sleep 1\n"
"s_cbranch_scc1 CH_init_p\n"
"s_branch CH_abort\n"
"CH_init_d:\n"
"v_mov_b64 v[32:33], 0\n"
"v_mov_b64 v[34:35], 0\n"
"v_mov_b64 v[36:37], 0\n"
"v_mov_b64 v[38:39], 0\n"
"v_mov_b64 v[40:41], 0\n"
"v_mov_b64 v[42:43], 0\n"
"v_mov_b64 v[44:45], 0\n"
"v_mov_b64 v[46:47], 0\n"
"s_waitcnt lgkmcnt(0)\n"
"s_mov_b64 s[64:65], s[72:73]\n"
"s_lshl_b32 s80, s3, 14\n"
"s_add_u32 s42, s64, s80\n"
"s_addc_u32 s43, s65, 0\n"
"s_add_u32 s44, s42, 1048576\n"
"s_addc_u32 s45, s43, 0\n"
"v_mov_b32 v28, 0x7f817f81\n"
"v_mov_b32 v29, 0x7f817f81\n"
"v_mov_b32 v30, 0x7f817f81\n"
"v_mov_b32 v31, 0x7f817f81\n"
"s_mov_b64 s[64:65], s[70:71]\n"
"s_add_u32 s66, s70, 131072\n"
"s_addc_u32 s67, s71, 0\n"
"s_lshl_b32 s80, s60, 14\n"
"s_add_u32 s80, s80, 4096\n"
"s_add_u32 s68, s70, s80\n"
"s_addc_u32 s69, s71, 0\n"
"s_memrealtime s[80:81]\n"
"s_waitcnt lgkmcnt(0)\n"
"s_mov_b32 s94, s80\n"
"CH_top:\n"
"s_waitcnt vmcnt(0)\n"
"CH_top2:\n"
"s_cmp_ge_u32 s3, 8\n"
"s_barrier\n"
"ds_read_b128 v[48:51], v2 offset:0\n"
"ds_read_b128 v[52:55], v2 offset:1024\n"
"ds_read_b128 v[56:59], v2 offset:2048\n"
"s_waitcnt lgkmcnt(2)\n"
"v_smfmac_f32_16x16x64_bf16 v[32:35], v[48:51], v[88:95], v1\n"
"ds_read_b128 v[60:63], v2 offset:3072\n"
"v_smfmac_f32_16x16x64_bf16 v[36:39], v[48:51], v[96:103], v1\n"
"ds_read_b128 v[64:67], v2 offset:4096\n"
"s_waitcnt lgkmcnt(3)\n"
"v_smfmac_f32_16x16x64_bf16 v[32:35], v[52:55], v[104:111], v1\n"
"ds_read_b128 v[68:71], v2 offset:5120\n"
"v_smfmac_f32_16x16x64_bf16 v[36:39], v[52:55], v[112:119], v1\n"
"ds_read_b128 v[72:75], v2 offset:6144\n"
"s_waitcnt lgkmcnt(4)\n"
"v_smfmac_f32_16x16x64_bf16 v[32:35], v[56:59], v[120:127], v1\n"
"ds_read_b128 v[76:79], v2 offset:7168\n"
"v_smfmac_f32_16x16x64_bf16 v[36:39], v[56:59], v[128:135], v1\n"
"global_store_dwordx4 v87, v[28:31], s[42:43] offset:0 sc1\n"
"s_waitcnt lgkmcnt(4)\n"
"v_smfmac_f32_16x16x64_bf16 v[32:35], v[60:63], v[136:143], v1\n"
"ds_read_b128 v[80:83], v2 offset:8192\n"
"v_smfmac_f32_16x16x64_bf16 v[36:39], v[60:63], v[144:151], v1\n"
"global_store_dwordx4 v87, v[28:31], s[42:43] offset:1024 sc1\n"
"s_waitcnt lgkmcnt(4)\n"
"v_smfmac_f32_16x16x64_bf16 v[32:35], v[64:67], v[152:159], v1\n"
"v_smfmac_f32_16x16x64_bf16 v[36:39], v[64:67], v[160:167], v1\n"
"s_cbranch_scc1 CH_one\n"
"global_store_dwordx4 v87, v[28:31], s[44:45] offset:0 sc1\n"
"global_store_dwordx4 v87, v[28:31], s[44:45] offset:1024 sc1\n"
"CH_one:\n"
"s_waitcnt lgkmcnt(3)\n"
"v_smfmac_f32_16x16x64_bf16 v[32:35], v[68:71], v[168:175], v1\n"
"ds_read_b128 v[88:91], v86 offset:0\n"
"v_smfmac_f32_16x16x64_bf16 v[36:39], v[68:71], v[176:183], v1\n"
"ds_read_b128 v[104:107], v86 offset:4096\n"
"s_waitcnt lgkmcnt(4)\n"
"v_smfmac_f32_16x16x64_bf16 v[32:35], v[72:75], v[184:191], v1\n"
"ds_read_b128 v[92:95], v86 offset:1024\n"
"v_smfmac_f32_16x16x64_bf16 v[36:39], v[72:75], v[192:199], v1\n"
"ds_read_b128 v[108:111], v86 offset:5120\n"
"s_waitcnt lgkmcnt(5)\n"
"v_smfmac_f32_16x16x64_bf16 v[32:35], v[76:79], v[200:207], v1\n"
"ds_read_b128 v[96:99], v86 offset:2048\n"
"v_smfmac_f32_16x16x64_bf16 v[36:39], v[76:79], v[208:215], v1\n"
"ds_read_b128 v[112:115], v86 offset:6144\n"
"s_waitcnt lgkmcnt(6)\n"
"v_smfmac_f32_16x16x64_bf16 v[32:35], v[80:83], v[216:223], v1\n"
"ds_read_b128 v[100:103], v86 offset:3072\n"
"v_smfmac_f32_16x16x64_bf16 v[36:39], v[80:83], v[224:231], v1\n"
"ds_read_b128 v[116:119], v86 offset:7168\n"
"ds_read_b128 v[24:27], v84\n"
"v_smfmac_f32_16x16x64_bf16 v[40:43], v[48:51], a[0:7], v1\n"
"v_smfmac_f32_16x16x64_bf16 v[44:47], v[48:51], a[8:15], v1\n"
"v_smfmac_f32_16x16x64_bf16 v[40:43], v[52:55], a[16:23], v1\n"
"v_permlane32_swap_b32 v32, v36\n"
"v_smfmac_f32_16x16x64_bf16 v[44:47], v[52:55], a[24:31], v1\n"
"v_permlane32_swap_b32 v33, v37\n"
"v_smfmac_f32_16x16x64_bf16 v[40:43], v[56:59], a[32:39], v1\n"
"v_add_f32 v32, v32, v36\n"
"v_smfmac_f32_16x16x64_bf16 v[44:47], v[56:59], a[40:47], v1\n"
"v_add_f32 v33, v33, v37\n"
"v_smfmac_f32_16x16x64_bf16 v[40:43], v[60:63], a[48:55], v1\n"
"v_permlane32_swap_b32 v34, v38\n"
"v_smfmac_f32_16x16x64_bf16 v[44:47], v[60:63], a[56:63], v1\n"
"v_permlane32_swap_b32 v35, v39\n"
"v_smfmac_f32_16x16x64_bf16 v[40:43], v[64:67], a[64:71], v1\n"
"v_cvt_pk_bf16_f32 v36, v32, v33\n"
"v_smfmac_f32_16x16x64_bf16 v[44:47], v[64:67], a[72:79], v1\n"
"v_add_f32 v34, v34, v38\n"
"v_smfmac_f32_16x16x64_bf16 v[40:43], v[68:71], a[80:87], v1\n"
"v_add_f32 v35, v35, v39\n"
"v_smfmac_f32_16x16x64_bf16 v[44:47], v[68:71], a[88:95], v1\n"
"v_cvt_pk_bf16_f32 v37, v34, v35\n"
"ds_write_b64 v4, v[36:37] offset:0\n"
"v_smfmac_f32_16x16x64_bf16 v[40:43], v[72:75], a[96:103], v1\n"
"v_cmp_u_f32 vcc, v32, v33\n"
"v_smfmac_f32_16x16x64_bf16 v[44:47], v[72:75], a[104:111], v1\n"
"v_cmp_u_f32 s[80:81], v34, v35\n"
"v_smfmac_f32_16x16x64_bf16 v[40:43], v[76:79], a[112:119], v1\n"
"v_mov_b64 v[164:165], 0\n"
"v_smfmac_f32_16x16x64_bf16 v[44:47], v[76:79], a[120:127], v1\n"
"s_or_b64 vcc, vcc, s[80:81]\n"
"s_waitcnt lgkmcnt(0)\n"
"s_cbranch_vccnz CH_nan_mem\n"
"CH_nan_ret:\n"
"s_barrier\n"
"v_smfmac_f32_16x16x64_bf16 v[40:43], v[80:83], a[128:135], v1\n"
"ds_read_b64_tr_b16 v[120:121], v5 offset:0\n"
"ds_read_b64_tr_b16 v[122:123], v5 offset:128\n"
"ds_read_b64_tr_b16 v[124:125], v5 offset:512\n"
"v_mov_b64 v[166:167], 0\n"
"v_smfmac_f32_16x16x64_bf16 v[44:47], v[80:83], a[136:143], v1\n"
"ds_read_b64_tr_b16 v[126:127], v5 offset:640\n"
"ds_read_b64_tr_b16 v[128:129], v5 offset:1024\n"
"ds_read_b64_tr_b16 v[130:131], v5 offset:1152\n"
"v_smfmac_f32_16x16x64_bf16 v[164:167], v[48:51], a[144:151], v1\n"
"ds_read_b64_tr_b16 v[132:133], v5 offset:1536\n"
"ds_read_b64_tr_b16 v[134:135], v5 offset:1664\n"
"v_smfmac_f32_16x16x64_bf16 v[164:167], v[52:55], a[152:159], v1\n"
"s_nop 0\n"
"v_permlane32_swap_b32 v40, v44\n"
"v_permlane32_swap_b32 v41, v45\n"
"v_add_f32 v40, v40, v44\n"
"v_add_f32 v41, v41, v45\n"
"v_permlane32_swap_b32 v42, v46\n"
"v_permlane32_swap_b32 v43, v47\n"
"v_cvt_pk_bf16_f32 v44, v40, v41\n"
"v_add_f32 v42, v42, v46\n"
"v_add_f32 v43, v43, v47\n"
"v_cvt_pk_bf16_f32 v45, v42, v43\n"
"ds_write_b64 v4, v[44:45] offset:2048\n"
"s_waitcnt lgkmcnt(7)\n"
"v_mfma_f32_16x16x32_bf16 v[152:155], v[88:91], v[120:123], 0\n"
"v_mfma_f32_16x16x32_bf16 v[156:159], v[104:107], v[120:123], 0\n"
"s_waitcnt lgkmcnt(5)\n"
"v_mfma_f32_16x16x32_bf16 v[152:155], v[92:95], v[124:127], v[152:155]\n"
"v_mfma_f32_16x16x32_bf16 v[156:159], v[108:111], v[124:127], v[156:159]\n"
"s_waitcnt lgkmcnt(0)\n"
"s_barrier\n"
"ds_read_b64_tr_b16 v[136:137], v5 offset:2048\n"
"ds_read_b64_tr_b16 v[138:139], v5 offset:2176\n"
"v_mfma_f32_16x16x32_bf16 v[152:155], v[96:99], v[128:131], v[152:155]\n"
"ds_read_b64_tr_b16 v[140:141], v5 offset:2560\n"
"ds_read_b64_tr_b16 v[142:143], v5 offset:2688\n"
"v_mfma_f32_16x16x32_bf16 v[156:159], v[112:115], v[128:131], v[156:159]\n"
"ds_read_b64_tr_b16 v[144:145], v5 offset:3072\n"
"ds_read_b64_tr_b16 v[146:147], v5 offset:3200\n"
"v_mfma_f32_16x16x32_bf16 v[152:155], v[100:103], v[132:135], v[152:155]\n"
"ds_read_b64_tr_b16 v[148:149], v5 offset:3584\n"
"ds_read_b64_tr_b16 v[150:151], v5 offset:3712\n"
"v_mfma_f32_16x16x32_bf16 v[156:159], v[116:119], v[132:135], v[156:159]\n"
"s_waitcnt lgkmcnt(6)\n"
"v_mfma_f32_16x16x32_bf16 v[160:163], v[136:139], v[120:123], 0\n"
"ds_read_b128 v[88:91], v3 offset:20480\n"
"s_waitcnt lgkmcnt(5)\n"
"v_mfma_f32_16x16x32_bf16 v[160:163], v[140:143], v[124:127], v[160:163]\n"
"ds_read_b128 v[92:95], v3 offset:21504\n"
"v_max3_f32 v112, v152, v153, v154\n"
"s_waitcnt lgkmcnt(4)\n"
"v_mfma_f32_16x16x32_bf16 v[160:163], v[144:147], v[128:131], v[160:163]\n"
"v_max3_f32 v112, v112, v155, v156\n"
"v_max3_f32 v112, v112, v157, v158\n"
"s_waitcnt lgkmcnt(2)\n"
"v_mfma_f32_16x16x32_bf16 v[160:163], v[148:151], v[132:135], v[160:163]\n"
"v_max_f32 v112, v112, v159\n"
"v_max_f32 v113, v112, v112\n"
"v_smfmac_f32_16x16x64_bf16 v[164:167], v[56:59], a[160:167], v1\n"
"ds_read_b128 v[96:99], v3 offset:22528\n"
"ds_read_b128 v[100:103], v3 offset:23552\n"
"v_permlane32_swap_b32 v112, v113\n"
"s_nop 1\n"
"v_max3_f32 v112, v112, v113, v160\n"
"v_max3_f32 v112, v112, v161, v162\n"
"v_max_f32 v113, v112, v163\n"
"v_max_f32 v112, v112, v163\n"
"v_smfmac_f32_16x16x64_bf16 v[164:167], v[60:63], a[168:175], v1\n"
"s_nop 0\n"
"v_permlane16_swap_b32 v112, v113\n"
"v_max_f32 v112, v112, v113\n"
"v_mul_f32 v112, s86, v112\n"
"v_smfmac_f32_16x16x64_bf16 v[164:167], v[64:67], a[176:183], v1\n"
"v_fma_f32 v152, v152, s84, v112\n"
"v_fma_f32 v153, v153, s84, v112\n"
"v_fma_f32 v154, v154, s84, v112\n"
"v_fma_f32 v155, v155, s84, v112\n"
"v_fma_f32 v156, v156, s84, v112\n"
"v_fma_f32 v157, v157, s84, v112\n"
"v_fma_f32 v158, v158, s84, v112\n"
"v_fma_f32 v159, v159, s84, v112\n"
"v_smfmac_f32_16x16x64_bf16 v[164:167], v[68:71], a[184:191], v1\n"
"v_exp_f32 v152, v152\n"
"v_exp_f32 v153, v153\n"
"v_exp_f32 v154, v154\n"
"v_exp_f32 v155, v155\n"
"v_smfmac_f32_16x16x64_bf16 v[164:167], v[72:75], a[192:199], v1\n"
"v_exp_f32 v156, v156\n"
"v_exp_f32 v157, v157\n"
"v_exp_f32 v158, v158\n"
"v_exp_f32 v159, v159\n"
"v_smfmac_f32_16x16x64_bf16 v[164:167], v[76:79], a[200:207], v1\n"
"v_fma_f32 v160, v160, s84, v112\n"
"v_fma_f32 v161, v161, s84, v112\n"
"v_fma_f32 v162, v162, s84, v112\n"
"v_fma_f32 v163, v163, s84, v112\n"
"v_smfmac_f32_16x16x64_bf16 v[164:167], v[80:83], a[208:215], v1\n"
"v_cvt_pk_bf16_f32 v104, v152, v153\n"
"v_cvt_pk_bf16_f32 v105, v154, v155\n"
"v_cvt_pk_bf16_f32 v106, v156, v157\n"
"v_cvt_pk_bf16_f32 v107, v158, v159\n"
"v_exp_f32 v160, v160\n"
"v_exp_f32 v161, v161\n"
"v_mfma_f32_16x16x32_bf16 v[36:39], v[16:19], v[104:107], 0\n"
"v_exp_f32 v162, v162\n"
"v_exp_f32 v163, v163\n"
"v_mfma_f32_16x16x32_bf16 v[32:35], v[24:27], v[104:107], 0\n"
"v_cvt_pk_bf16_f32 v108, v160, v161\n"
"v_cvt_pk_bf16_f32 v109, v162, v163\n"
"v_cvt_pk_bf16_f32 v110, v164, v165\n"
"v_cvt_pk_bf16_f32 v111, v166, v167\n"
"v_mfma_f32_16x16x16_bf16 v[36:39], v[14:15], v[108:109], v[36:39]\n"
"v_mfma_f32_16x16x16_bf16 v[32:35], v[110:111], v[108:109], v[32:35]\n"
"v_mov_b64 v[48:49], 0\n"
"v_mov_b64 v[50:51], 0\n"
"v_mov_b64 v[52:53], 0\n"
"v_mov_b64 v[54:55], 0\n"
"v_mov_b64 v[56:57], 0\n"
"v_rcp_f32 v113, v36\n"
"v_mov_b64 v[58:59], 0\n"
"v_mov_b64 v[60:61], 0\n"
"s_cmp_eq_u32 s87, 63\n"
"v_mul_f32 v32, v32, v113\n"
"v_mul_f32 v33, v33, v113\n"
"v_mul_f32 v34, v34, v113\n"
"v_mul_f32 v35, v35, v113\n"
"v_cvt_pk_bf16_f32 v32, v32, v33\n"
"v_cvt_pk_bf16_f32 v33, v34, v35\n"
"ds_write_b64 v6, v[32:33]\n"
"s_cbranch_scc1 CH_last\n"
"v_mov_b64 v[62:63], 0\n"
"v_mov_b64 v[64:65], 0\n"
"v_mov_b64 v[66:67], 0\n"
"v_mfma_f32_16x16x32_bf16 v[152:155], v[16:19], v[16:19], v[152:155]\n"
"v_mfma_f32_16x16x32_bf16 v[152:155], v[16:19], v[16:19], v[152:155]\n"
"v_mfma_f32_16x16x32_bf16 v[152:155], v[16:19], v[16:19], v[152:155]\n"
"s_cmp_ge_u32 s58, 2\n"
"s_waitcnt vmcnt(0) lgkmcnt(0)\n"
"s_barrier\n"
"ds_read_b128 v[120:123], v7 offset:0\n"
"ds_read_b128 v[124:127], v7 offset:512\n"
"v_mfma_f32_16x16x32_bf16 v[152:155], v[16:19], v[16:19], v[152:155]\n"
"v_mfma_f32_16x16x32_bf16 v[152:155], v[16:19], v[16:19], v[152:155]\n"
"v_mfma_f32_16x16x32_bf16 v[152:155], v[16:19], v[16:19], v[152:155]\n"
"s_waitcnt lgkmcnt(1)\n"
"v_smfmac_f32_16x16x64_bf16 v[48:51], v[120:123], a[216:223], v1\n"
"v_smfmac_f32_16x16x64_bf16 v[52:55], v[120:123], a[232:239], v1\n"
"s_waitcnt lgkmcnt(0)\n"
"v_smfmac_f32_16x16x64_bf16 v[48:51], v[124:127], a[224:231], v1\n"
"v_smfmac_f32_16x16x64_bf16 v[52:55], v[124:127], a[240:247], v1\n"
"v_smfmac_f32_16x16x64_bf16 v[56:59], v[120:123], a[248:255], v1\n"
"v_smfmac_f32_16x16x64_bf16 v[60:63], v[120:123], v[240:247], v1\n"
"v_cvt_pk_bf16_f32 v68, v48, v49\n"
"v_smfmac_f32_16x16x64_bf16 v[56:59], v[124:127], v[232:239], v1\n"
"v_cvt_pk_bf16_f32 v70, v52, v53\n"
"v_smfmac_f32_16x16x64_bf16 v[60:63], v[124:127], v[248:255], v1\n"
"v_cvt_pk_bf16_f32 v69, v50, v51\n"
"v_smfmac_f32_16x16x64_bf16 v[64:67], v[120:123], v[88:95], v1\n"
"v_cvt_pk_bf16_f32 v71, v54, v55\n"
"v_smfmac_f32_16x16x64_bf16 v[64:67], v[124:127], v[96:103], v1\n"
"v_permlane16_swap_b32 v68, v70\n"
"v_permlane16_swap_b32 v69, v71\n"
"global_store_dwordx4 v8, v[68:71], s[64:65] sc1\n"
"v_cvt_pk_bf16_f32 v72, v56, v57\n"
"v_cvt_pk_bf16_f32 v74, v60, v61\n"
"v_cvt_pk_bf16_f32 v73, v58, v59\n"
"v_cvt_pk_bf16_f32 v75, v62, v63\n"
"v_permlane16_swap_b32 v72, v74\n"
"s_nop 0\n"
"v_permlane16_swap_b32 v73, v75\n"
"global_store_dwordx4 v8, v[72:75], s[66:67] sc1\n"
"s_cbranch_scc1 CH_no5\n"
"v_cvt_pk_bf16_f32 v76, v64, v65\n"
"v_cvt_pk_bf16_f32 v77, v66, v67\n"
"global_store_dwordx2 v9, v[76:77], s[64:65] sc1\n"
"CH_no5:\n"
"s_cmp_eq_u32 s59, 0\n"
"s_cbranch_scc1 CH_other\n"
"s_memrealtime s[6:7]\n"
"v_mov_b32 v28, v164\n"
"v_mov_b32 v29, v165\n"
"v_mov_b32 v30, v166\n"
"v_mov_b32 v31, v167\n"
"v_permlane32_swap_b32 v28, v164\n"
"v_permlane32_swap_b32 v29, v165\n"
"v_permlane32_swap_b32 v30, v166\n"
"v_permlane32_swap_b32 v31, v167\n"
"v_add_f32 v28, v28, v164\n"
"v_add_f32 v29, v29, v165\n"
"v_add_f32 v30, v30, v166\n"
"v_add_f32 v31, v31, v167\n"
"v_cvt_pk_bf16_f32 v28, v28, v29\n"
"v_cvt_pk_bf16_f32 v29, v30, v31\n"
"s_mov_b32 exec_hi, 0\n"
"global_store_dwordx2 v12, v[28:29], s[54:55] sc1\n"
"s_mov_b64 exec, -1\n"
"v_readfirstlane_b32 s91, v85\n"
"ds_read_b128 v[88:91], v3 offset:0\n"
"ds_read_b128 v[92:95], v3 offset:1024\n"
"ds_read_b128 v[96:99], v3 offset:2048\n"
"ds_read_b128 v[100:103], v3 offset:3072\n"
"ds_read_b128 v[104:107], v3 offset:4096\n"
"ds_read_b128 v[108:111], v3 offset:5120\n"
"ds_read_b128 v[112:115], v3 offset:6144\n"
"ds_read_b128 v[116:119], v3 offset:7168\n"
"ds_read_b128 v[120:123], v3 offset:8192\n"
"ds_read_b128 v[124:127], v3 offset:9216\n"
"ds_read_b128 v[128:131], v3 offset:10240\n"
"ds_read_b128 v[132:135], v3 offset:11264\n"
"ds_read_b128 v[136:139], v3 offset:12288\n"
"ds_read_b128 v[140:143], v3 offset:13312\n"
"ds_read_b128 v[144:147], v3 offset:14336\n"
"ds_read_b128 v[148:151], v3 offset:15360\n"
"ds_read_b128 v[152:155], v3 offset:16384\n"
"ds_read_b128 v[156:159], v3 offset:17408\n"
"ds_read_b128 v[160:163], v3 offset:18432\n"
"ds_read_b128 v[164:167], v3 offset:19456\n"
"v_mov_b64 v[32:33], 0\n"
"v_mov_b64 v[34:35], 0\n"
"v_mov_b64 v[36:37], 0\n"
"v_mov_b64 v[38:39], 0\n"
"v_mov_b64 v[40:41], 0\n"
"v_mov_b64 v[42:43], 0\n"
"v_mov_b64 v[44:45], 0\n"
"v_mov_b64 v[46:47], 0\n"
"s_add_u32 s54, s54, 131072\n"
"s_addc_u32 s55, s55, 0\n"
"s_add_u32 s87, s87, 1\n"
"s_mov_b64 s[40:41], s[68:69]\n"
"s_lshl_b32 s80, s3, 14\n"
"s_add_u32 s42, s64, s80\n"
"s_addc_u32 s43, s65, 0\n"
"s_add_u32 s44, s42, 1048576\n"
"s_addc_u32 s45, s43, 0\n"
"s_mov_b64 s[80:81], s[70:71]\n"
"s_mov_b64 s[70:71], s[72:73]\n"
"s_mov_b64 s[72:73], s[80:81]\n"
"s_mov_b64 s[64:65], s[70:71]\n"
"s_add_u32 s66, s70, 131072\n"
"s_addc_u32 s67, s71, 0\n"
"s_lshl_b32 s80, s60, 14\n"
"s_add_u32 s80, s80, 4096\n"
"s_add_u32 s68, s70, s80\n"
"s_addc_u32 s69, s71, 0\n"
"s_lshl_b32 s82, s58, 11\n"
"s_add_u32 s4, s76, s82\n"
"s_addc_u32 s5, s77, 0\n"
"s_mov_b32 m0, s82\n"
"v_xor_b32 v86, 49152, v86\n"
"v_xor_b32 v84, 49152, v84\n"
"s_waitcnt lgkmcnt(0)\n"
"s_sub_i32 s80, s6, s94\n"
"s_cmp_eq_u32 s89, 0\n"
"s_cselect_b32 s80, s80, s95\n"
"s_cmp_eq_u32 s95, 0\n"
"s_cselect_b32 s95, 110, s80\n"
"s_add_i32 s80, s6, 30\n"
"s_cmp_eq_u32 s91, 0x7f817f81\n"
"s_cselect_b32 s91, s80, s91\n"
"s_sub_i32 s82, s91, s6\n"
"s_max_i32 s82, s82, 8\n"
"s_min_i32 s82, s82, 120\n"
"s_add_i32 s91, s6, s82\n"
"s_add_i32 s94, s91, 56\n"
"s_add_i32 s90, s94, s95\n"
"s_add_i32 s90, s90, 20\n"
"s_mov_b32 s89, 0\n"
"s_and_b32 s82, s91, -4\n"
"s_sub_i32 s82, s82, 1\n"
"CH_w1_c:\n"
"s_memrealtime s[80:81]\n"
"s_waitcnt lgkmcnt(0)\n"
"s_sub_i32 s80, s82, s80\n"
"s_cmp_gt_i32 s80, 0\n"
"s_cbranch_scc1 CH_w1_c\n"
"CH_red:\n"
"global_load_dwordx4 v[48:51], v2, s[40:41] offset:-4096 sc1\n"
"global_load_dwordx4 v[52:55], v2, s[40:41] offset:-3072 sc1\n"
"global_load_dwordx4 v[56:59], v2, s[40:41] offset:-2048 sc1\n"
"global_load_dwordx4 v[60:63], v2, s[40:41] offset:-1024 sc1\n"
"global_load_dwordx4 v[64:67], v2, s[40:41] offset:0 sc1\n"
"global_load_dwordx4 v[68:71], v2, s[40:41] offset:1024 sc1\n"
"global_load_dwordx4 v[72:75], v2, s[40:41] offset:2048 sc1\n"
"global_load_dwordx4 v[76:79], v2, s[40:41] offset:3072 sc1\n"
"s_waitcnt vmcnt(7)\n"
"v_mfma_f32_16x16x32_bf16 v[80:83], v[48:51], v[20:23], 0\n"
"s_waitcnt vmcnt(6)\n"
"v_mfma_f32_16x16x32_bf16 v[80:83], v[52:55], v[20:23], v[80:83]\n"
"s_waitcnt vmcnt(5)\n"
"v_mfma_f32_16x16x32_bf16 v[80:83], v[56:59], v[20:23], v[80:83]\n"
"s_waitcnt vmcnt(4)\n"
"v_mfma_f32_16x16x32_bf16 v[80:83], v[60:63], v[20:23], v[80:83]\n"
"s_waitcnt vmcnt(3)\n"
"v_mfma_f32_16x16x32_bf16 v[80:83], v[64:67], v[20:23], v[80:83]\n"
"s_waitcnt vmcnt(2)\n"
"v_mfma_f32_16x16x32_bf16 v[80:83], v[68:71], v[20:23], v[80:83]\n"
"s_waitcnt vmcnt(1)\n"
"v_mfma_f32_16x16x32_bf16 v[80:83], v[72:75], v[20:23], v[80:83]\n"
"s_waitcnt vmcnt(0)\n"
"v_mfma_f32_16x16x32_bf16 v[80:83], v[76:79], v[20:23], v[80:83]\n"
"s_nop 8\n"
"v_permlane32_swap_b32 v80, v82\n"
"v_permlane32_swap_b32 v81, v83\n"
"v_add_f32 v80, v80, v82\n"
"v_add_f32 v81, v81, v83\n"
"s_mov_b64 exec, s[92:93]\n"
"v_cvt_pk_bf16_f32 v82, v80, v81\n"
"v_cmpx_o_f32 vcc, v80, v81\n"
"global_store_dword v10, v82, s[76:77] sc1\n"
"s_cmp_eq_u64 vcc, s[92:93]\n"
"s_mov_b64 exec, -1\n"
"s_cbranch_scc0 CH_nan_red\n"
"CH_red_ret:\n"
"s_cmp_eq_u32 s89, 0\n"
"s_cbranch_scc1 CH_ontime\n"
"s_memrealtime s[80:81]\n"
"s_waitcnt lgkmcnt(0)\n"
"s_add_i32 s94, s80, 56\n"
"s_add_i32 s90, s94, s95\n"
"s_add_i32 s90, s90, 76\n"
"CH_ontime:\n"
"v_mov_b32 v28, 0x7f817f81\n"
"v_mov_b32 v29, 0x7f817f81\n"
"v_mov_b32 v30, 0x7f817f81\n"
"v_mov_b32 v31, 0x7f817f81\n"
"s_cmp_eq_u32 s63, 0\n"
"s_cbranch_scc1 CH_nolead\n"
"v_mov_b32 v80, s90\n"
"v_mov_b32 v81, 17408\n"
"s_mov_b64 exec, 1\n"
"global_store_dword v81, v80, s[76:77] sc1\n"
"global_store_dword v81, v28, s[78:79] sc1\n"
"s_mov_b64 exec, -1\n"
"CH_nolead:\n"
"s_cmp_lg_u32 s58, 0\n"
"s_cbranch_scc1 CH_w2_e\n"
"s_and_b32 s82, s94, -4\n"
"s_sub_i32 s82, s82, 1\n"
"CH_w2_c:\n"
"s_memrealtime s[80:81]\n"
"s_waitcnt lgkmcnt(0)\n"
"s_sub_i32 s80, s82, s80\n"
"s_cmp_gt_i32 s80, 0\n"
"s_cbranch_scc1 CH_w2_c\n"
"CH_w2_e:\n"
"s_barrier\n"
"global_load_lds_dwordx4 v2, s[4:5] sc1\n"
"global_load_lds_dwordx4 v2, s[4:5] offset:1024 sc1\n"
"s_add_u32 s82, s76, 17408\n"
"s_addc_u32 s83, s77, 0\n"
"v_mov_b32 v80, 0\n"
"global_load_dword v85, v80, s[82:83] sc1\n"
"s_mov_b64 exec, s[92:93]\n"
"global_store_dword v10, v28, s[78:79] sc1\n"
"s_mov_b64 exec, -1\n"
"v_mov_b32 v28, 0\n"
"v_mov_b32 v29, 1\n"
"s_mov_b64 exec, 1\n"
"global_atomic_add v28, v29, s[98:99] sc1\n"
"s_mov_b64 exec, -1\n"
"s_mov_b64 s[80:81], s[74:75]\n"
"s_mov_b64 s[74:75], s[76:77]\n"
"s_mov_b64 s[76:77], s[78:79]\n"
"s_mov_b64 s[78:79], s[80:81]\n"
"s_mov_b32 s89, 0\n"
"v_mov_b32 v28, 0x7f817f81\n"
"v_mov_b32 v29, 0x7f817f81\n"
"v_mov_b32 v30, 0x7f817f81\n"
"v_mov_b32 v31, 0x7f817f81\n"
"s_waitcnt vmcnt(2) lgkmcnt(0)\n"
"s_branch CH_top2\n"
"CH_other:\n"
"v_mov_b32 v28, v164\n"
"v_mov_b32 v29, v165\n"
"v_mov_b32 v30, v166\n"
"v_mov_b32 v31, v167\n"
"v_permlane32_swap_b32 v28, v164\n"
"v_permlane32_swap_b32 v29, v165\n"
"v_permlane32_swap_b32 v30, v166\n"
"v_permlane32_swap_b32 v31, v167\n"
"v_add_f32 v28, v28, v164\n"
"v_add_f32 v29, v29, v165\n"
"v_add_f32 v30, v30, v166\n"
"v_add_f32 v31, v31, v167\n"
"v_cvt_pk_bf16_f32 v28, v28, v29\n"
"v_cvt_pk_bf16_f32 v29, v30, v31\n"
"s_mov_b32 exec_hi, 0\n"
"global_store_dwordx2 v12, v[28:29], s[54:55] sc1\n"
"s_mov_b64 exec, -1\n"
"s_cmp_lt_u32 s58, 2\n"
"s_cbranch_scc1 CH_nokv\n"
"s_cmp_lg_u32 s61, 0\n"
"s_cbranch_scc1 CH_nokm\n"
"ds_read_b64_tr_b16 v[48:49], v13 offset:0\n"
"ds_read_b64_tr_b16 v[50:51], v13 offset:128\n"
"ds_read_b64_tr_b16 v[52:53], v13 offset:512\n"
"ds_read_b64_tr_b16 v[54:55], v13 offset:640\n"
"s_waitcnt lgkmcnt(0)\n"
"s_mov_b64 exec, s[92:93]\n"
"global_store_dwordx4 v11, v[48:51], s[54:55] offset:0 sc1\n"
"global_store_dwordx4 v11, v[52:55], s[54:55] offset:512 sc1\n"
"s_mov_b64 exec, -1\n"
"s_nop 0\n"
"CH_nokm:\n"
"s_cmp_lg_u32 s87, 0\n"
"s_cbranch_scc1 CH_nokv\n"
"s_mov_b32 s89, 0\n"
"v_mov_b32 v28, 0\n"
"CH_kv1_p:\n"
"global_load_dword v29, v28, s[96:97] sc1\n"
"s_waitcnt vmcnt(0)\n"
"v_readfirstlane_b32 s88, v29\n"
"s_nop 3\n"
"s_cmp_ge_u32 s88, 8\n"
"s_cbranch_scc1 CH_kv1_d\n"
"s_add_u32 s89, s89, 1\n"
"s_cmp_lt_u32 s89, 0x100000\n"
"s_sleep 1\n"
"s_cbranch_scc1 CH_kv1_p\n"
"s_branch CH_abort\n"
"CH_kv1_d:\n"
"s_sub_u32 s82, s58, 2\n"
"s_lshl_b32 s82, s82, 12\n"
"s_add_u32 s80, s52, s82\n"
"s_addc_u32 s81, s53, 0\n"
"s_add_u32 s82, s82, s2\n"
"s_mov_b32 m0, s82\n"
"s_nop 0\n"
"global_load_lds_dwordx4 v2, s[80:81] sc1\n"
"global_load_lds_dwordx4 v2, s[80:81] offset:1024 sc1\n"
"global_load_lds_dwordx4 v2, s[80:81] offset:2048 sc1\n"
"global_load_lds_dwordx4 v2, s[80:81] offset:3072 sc1\n"
"s_sub_u32 s82, s58, 2\n"
"s_lshl_b32 s82, s82, 11\n"
"s_add_u32 s82, s82, 8192\n"
"s_add_u32 s83, s82, s2\n"
"s_mov_b32 m0, s83\n"
"s_lshl_b32 s83, s61, 12\n"
"s_add_u32 s82, s82, s83\n"
"s_add_u32 s80, s52, s82\n"
"s_addc_u32 s81, s53, 0\n"
"global_load_lds_dwordx4 v2, s[80:81] sc1\n"
"global_load_lds_dwordx4 v2, s[80:81] offset:1024 sc1\n"
"s_add_u32 s52, s52, 524288\n"
"s_addc_u32 s53, s53, 0\n"
"s_xor_b32 s2, s2, 49152\n"
"CH_nokv:\n"
"ds_read_b128 v[88:91], v3 offset:0\n"
"ds_read_b128 v[92:95], v3 offset:1024\n"
"ds_read_b128 v[96:99], v3 offset:2048\n"
"ds_read_b128 v[100:103], v3 offset:3072\n"
"ds_read_b128 v[104:107], v3 offset:4096\n"
"ds_read_b128 v[108:111], v3 offset:5120\n"
"ds_read_b128 v[112:115], v3 offset:6144\n"
"ds_read_b128 v[116:119], v3 offset:7168\n"
"ds_read_b128 v[120:123], v3 offset:8192\n"
"ds_read_b128 v[124:127], v3 offset:9216\n"
"ds_read_b128 v[128:131], v3 offset:10240\n"
"ds_read_b128 v[132:135], v3 offset:11264\n"
"ds_read_b128 v[136:139], v3 offset:12288\n"
"ds_read_b128 v[140:143], v3 offset:13312\n"
"ds_read_b128 v[144:147], v3 offset:14336\n"
"ds_read_b128 v[148:151], v3 offset:15360\n"
"ds_read_b128 v[152:155], v3 offset:16384\n"
"ds_read_b128 v[156:159], v3 offset:17408\n"
"ds_read_b128 v[160:163], v3 offset:18432\n"
"ds_read_b128 v[164:167], v3 offset:19456\n"
"v_mov_b64 v[32:33], 0\n"
"v_mov_b64 v[34:35], 0\n"
"v_mov_b64 v[36:37], 0\n"
"v_mov_b64 v[38:39], 0\n"
"v_mov_b64 v[40:41], 0\n"
"v_mov_b64 v[42:43], 0\n"
"v_mov_b64 v[44:45], 0\n"
"v_mov_b64 v[46:47], 0\n"
"s_add_u32 s54, s54, 131072\n"
"s_addc_u32 s55, s55, 0\n"
"s_add_u32 s87, s87, 1\n"
"s_mov_b64 s[40:41], s[68:69]\n"
"s_lshl_b32 s80, s3, 14\n"
"s_add_u32 s42, s64, s80\n"
"s_addc_u32 s43, s65, 0\n"
"s_add_u32 s44, s42, 1048576\n"
"s_addc_u32 s45, s43, 0\n"
"s_mov_b64 s[80:81], s[70:71]\n"
"s_mov_b64 s[70:71], s[72:73]\n"
"s_mov_b64 s[72:73], s[80:81]\n"
"s_mov_b64 s[64:65], s[70:71]\n"
"s_add_u32 s66, s70, 131072\n"
"s_addc_u32 s67, s71, 0\n"
"s_lshl_b32 s80, s60, 14\n"
"s_add_u32 s80, s80, 4096\n"
"s_add_u32 s68, s70, s80\n"
"s_addc_u32 s69, s71, 0\n"
"s_lshl_b32 s82, s58, 11\n"
"s_add_u32 s4, s76, s82\n"
"s_addc_u32 s5, s77, 0\n"
"s_mov_b32 m0, s82\n"
"v_xor_b32 v86, 49152, v86\n"
"v_xor_b32 v84, 49152, v84\n"
"s_waitcnt vmcnt(0) lgkmcnt(0)\n"
"v_mov_b32 v28, 0\n"
"v_mov_b32 v29, 1\n"
"s_mov_b64 exec, 1\n"
"global_atomic_add v28, v29, s[98:99] sc1\n"
"s_mov_b64 exec, -1\n"
"s_cmp_lt_u32 s58, 2\n"
"s_cbranch_scc1 CH_kvn_d\n"
"s_lshl_b32 s82, s87, 2\n"
"s_add_u32 s82, s82, 8\n"
"s_min_u32 s82, s82, 256\n"
"s_cmp_ge_u32 s88, s82\n"
"s_cbranch_scc1 CH_kvn_d\n"
"s_mov_b32 s89, 0\n"
"v_mov_b32 v28, 0\n"
"CH_kvn_p:\n"
"global_load_dword v29, v28, s[96:97] sc1\n"
"s_waitcnt vmcnt(0)\n"
"v_readfirstlane_b32 s88, v29\n"
"s_nop 3\n"
"s_cmp_ge_u32 s88, s82\n"
"s_cbranch_scc1 CH_kvn_d\n"
"s_add_u32 s89, s89, 1\n"
"s_cmp_lt_u32 s89, 0x100000\n"
"s_sleep 1\n"
"s_cbranch_scc1 CH_kvn_p\n"
"s_branch CH_abort\n"
"CH_kvn_d:\n"
"s_barrier\n"
"global_load_lds_dwordx4 v2, s[4:5] sc1\n"
"global_load_lds_dwordx4 v2, s[4:5] offset:1024 sc1\n"
"s_cmp_lg_u32 s58, 3\n"
"s_cbranch_scc1 CH_md1\n"
"s_add_u32 s80, s76, 16384\n"
"s_addc_u32 s81, s77, 0\n"
"s_mov_b32 m0, 8192\n"
"s_nop 0\n"
"global_load_lds_dwordx4 v2, s[80:81] sc1\n"
"CH_md1:\n"
"s_cmp_lt_u32 s58, 2\n"
"s_cbranch_scc1 CH_nokv2\n"
"s_cmp_ge_u32 s87, 63\n"
"s_cbranch_scc1 CH_nokv2\n"
"s_sub_u32 s82, s58, 2\n"
"s_lshl_b32 s82, s82, 12\n"
"s_add_u32 s80, s52, s82\n"
"s_addc_u32 s81, s53, 0\n"
"s_add_u32 s82, s82, s2\n"
"s_mov_b32 m0, s82\n"
"s_nop 0\n"
"global_load_lds_dwordx4 v2, s[80:81] sc1\n"
"global_load_lds_dwordx4 v2, s[80:81] offset:1024 sc1\n"
"global_load_lds_dwordx4 v2, s[80:81] offset:2048 sc1\n"
"global_load_lds_dwordx4 v2, s[80:81] offset:3072 sc1\n"
"s_sub_u32 s82, s58, 2\n"
"s_lshl_b32 s82, s82, 11\n"
"s_add_u32 s82, s82, 8192\n"
"s_add_u32 s83, s82, s2\n"
"s_mov_b32 m0, s83\n"
"s_lshl_b32 s83, s61, 12\n"
"s_add_u32 s82, s82, s83\n"
"s_add_u32 s80, s52, s82\n"
"s_addc_u32 s81, s53, 0\n"
"global_load_lds_dwordx4 v2, s[80:81] sc1\n"
"global_load_lds_dwordx4 v2, s[80:81] offset:1024 sc1\n"
"s_add_u32 s52, s52, 524288\n"
"s_addc_u32 s53, s53, 0\n"
"s_xor_b32 s2, s2, 49152\n"
"s_mov_b64 s[80:81], s[74:75]\n"
"s_mov_b64 s[74:75], s[76:77]\n"
"s_mov_b64 s[76:77], s[78:79]\n"
"s_mov_b64 s[78:79], s[80:81]\n"
"s_mov_b32 s89, 0\n"
"v_mov_b32 v28, 0x7f817f81\n"
"v_mov_b32 v29, 0x7f817f81\n"
"v_mov_b32 v30, 0x7f817f81\n"
"v_mov_b32 v31, 0x7f817f81\n"
"s_waitcnt vmcnt(6) lgkmcnt(0)\n"
"s_branch CH_top2\n"
"CH_nokv2:\n"
"s_mov_b64 s[80:81], s[74:75]\n"
"s_mov_b64 s[74:75], s[76:77]\n"
"s_mov_b64 s[76:77], s[78:79]\n"
"s_mov_b64 s[78:79], s[80:81]\n"
"s_mov_b32 s89, 0\n"
"v_mov_b32 v28, 0x7f817f81\n"
"v_mov_b32 v29, 0x7f817f81\n"
"v_mov_b32 v30, 0x7f817f81\n"
"v_mov_b32 v31, 0x7f817f81\n"
"s_waitcnt vmcnt(0) lgkmcnt(0)\n"
"s_branch CH_top2\n"
"CH_nan_mem:\n"
"s_cmp_eq_u32 s87, 0\n"
"s_cbranch_scc1 CH_nan_ret\n"
"v_max3_i32 v28, v48, v49, v50\n"
"v_max3_i32 v29, v51, v52, v53\n"
"v_max3_i32 v28, v28, v54, v55\n"
"v_max3_i32 v29, v29, v56, v57\n"
"v_max3_i32 v28, v28, v58, v59\n"
"v_max3_i32 v29, v29, v60, v61\n"
"v_max3_i32 v28, v28, v62, v63\n"
"v_max3_i32 v29, v29, v64, v65\n"
"v_max3_i32 v28, v28, v66, v67\n"
"v_max3_i32 v29, v29, v68, v69\n"
"v_max3_i32 v28, v28, v70, v71\n"
"v_max3_i32 v29, v29, v72, v73\n"
"v_max3_i32 v28, v28, v74, v75\n"
"v_max3_i32 v29, v29, v76, v77\n"
"v_max3_i32 v28, v28, v78, v79\n"
"v_max3_i32 v29, v29, v80, v81\n"
"v_max3_i32 v28, v28, v82, v83\n"
"v_max_i32 v28, v28, v29\n"
"v_cmp_lt_i32 vcc, 0x7f80ffff, v28\n"
"s_add_u32 s89, s89, 1\n"
"s_cbranch_vccz CH_nan_ret\n"
"s_and_b32 s80, s89, 63\n"
"s_cbranch_scc1 CH_mem_again\n"
"s_mov_b64 s[80:81], 0\n"
"v_mov_b32 v28, 0x7f817f81\n"
"v_cmp_eq_u32 vcc, v28, v48\n"
"s_nop 4\n"
"s_or_b64 s[80:81], s[80:81], vcc\n"
"v_cmp_eq_u32 vcc, v28, v49\n"
"s_nop 4\n"
"s_or_b64 s[80:81], s[80:81], vcc\n"
"v_cmp_eq_u32 vcc, v28, v50\n"
"s_nop 4\n"
"s_or_b64 s[80:81], s[80:81], vcc\n"
"v_cmp_eq_u32 vcc, v28, v51\n"
"s_nop 4\n"
"s_or_b64 s[80:81], s[80:81], vcc\n"
"v_cmp_eq_u32 vcc, v28, v52\n"
"s_nop 4\n"
"s_or_b64 s[80:81], s[80:81], vcc\n"
"v_cmp_eq_u32 vcc, v28, v53\n"
"s_nop 4\n"
"s_or_b64 s[80:81], s[80:81], vcc\n"
"v_cmp_eq_u32 vcc, v28, v54\n"
"s_nop 4\n"
"s_or_b64 s[80:81], s[80:81], vcc\n"
"v_cmp_eq_u32 vcc, v28, v55\n"
"s_nop 4\n"
"s_or_b64 s[80:81], s[80:81], vcc\n"
"v_cmp_eq_u32 vcc, v28, v56\n"
"s_nop 4\n"
"s_or_b64 s[80:81], s[80:81], vcc\n"
"v_cmp_eq_u32 vcc, v28, v57\n"
"s_nop 4\n"
"s_or_b64 s[80:81], s[80:81], vcc\n"
"v_cmp_eq_u32 vcc, v28, v58\n"
"s_nop 4\n"
"s_or_b64 s[80:81], s[80:81], vcc\n"
"v_cmp_eq_u32 vcc, v28, v59\n"
"s_nop 4\n"
"s_or_b64 s[80:81], s[80:81], vcc\n"
"v_cmp_eq_u32 vcc, v28, v60\n"
"s_nop 4\n"
"s_or_b64 s[80:81], s[80:81], vcc\n"
"v_cmp_eq_u32 vcc, v28, v61\n"
"s_nop 4\n"
"s_or_b64 s[80:81], s[80:81], vcc\n"
"v_cmp_eq_u32 vcc, v28, v62\n"
"s_nop 4\n"
"s_or_b64 s[80:81], s[80:81], vcc\n"
"v_cmp_eq_u32 vcc, v28, v63\n"
"s_nop 4\n"
"s_or_b64 s[80:81], s[80:81], vcc\n"
"v_cmp_eq_u32 vcc, v28, v64\n"
"s_nop 4\n"
"s_or_b64 s[80:81], s[80:81], vcc\n"
"v_cmp_eq_u32 vcc, v28, v65\n"
"s_nop 4\n"
"s_or_b64 s[80:81], s[80:81], vcc\n"
"v_cmp_eq_u32 vcc, v28, v66\n"
"s_nop 4\n"
"s_or_b64 s[80:81], s[80:81], vcc\n"
"v_cmp_eq_u32 vcc, v28, v67\n"
"s_nop 4\n"
"s_or_b64 s[80:81], s[80:81], vcc\n"
"v_cmp_eq_u32 vcc, v28, v68\n"
"s_nop 4\n"
"s_or_b64 s[80:81], s[80:81], vcc\n"
"v_cmp_eq_u32 vcc, v28, v69\n"
"s_nop 4\n"
"s_or_b64 s[80:81], s[80:81], vcc\n"
"v_cmp_eq_u32 vcc, v28, v70\n"
"s_nop 4\n"
"s_or_b64 s[80:81], s[80:81], vcc\n"
"v_cmp_eq_u32 vcc, v28, v71\n"
"s_nop 4\n"
"s_or_b64 s[80:81], s[80:81], vcc\n"
"v_cmp_eq_u32 vcc, v28, v72\n"
"s_nop 4\n"
"s_or_b64 s[80:81], s[80:81], vcc\n"
"v_cmp_eq_u32 vcc, v28, v73\n"
"s_nop 4\n"
"s_or_b64 s[80:81], s[80:81], vcc\n"
"v_cmp_eq_u32 vcc, v28, v74\n"
"s_nop 4\n"
"s_or_b64 s[80:81], s[80:81], vcc\n"
"v_cmp_eq_u32 vcc, v28, v75\n"
"s_nop 4\n"
"s_or_b64 s[80:81], s[80:81], vcc\n"
"v_cmp_eq_u32 vcc, v28, v76\n"
"s_nop 4\n"
"s_or_b64 s[80:81], s[80:81], vcc\n"
"v_cmp_eq_u32 vcc, v28, v77\n"
"s_nop 4\n"
"s_or_b64 s[80:81], s[80:81], vcc\n"
"v_cmp_eq_u32 vcc, v28, v78\n"
"s_nop 4\n"
"s_or_b64 s[80:81], s[80:81], vcc\n"
"v_cmp_eq_u32 vcc, v28, v79\n"
"s_nop 4\n"
"s_or_b64 s[80:81], s[80:81], vcc\n"
"v_cmp_eq_u32 vcc, v28, v80\n"
"s_nop 4\n"
"s_or_b64 s[80:81], s[80:81], vcc\n"
"v_cmp_eq_u32 vcc, v28, v81\n"
"s_nop 4\n"
"s_or_b64 s[80:81], s[80:81], vcc\n"
"v_cmp_eq_u32 vcc, v28, v82\n"
"s_nop 4\n"
"s_or_b64 s[80:81], s[80:81], vcc\n"
"v_cmp_eq_u32 vcc, v28, v83\n"
"s_nop 4\n"
"s_or_b64 s[80:81], s[80:81], vcc\n"
"s_cmp_eq_u64 s[80:81], 0\n"
"s_cbranch_scc1 CH_nan_ret\n"
"s_cmp_ge_u32 s89, 0x40000\n"
"s_cbranch_scc1 CH_abort\n"
"CH_mem_again:\n"
"s_lshl_b32 s82, s58, 11\n"
"s_add_u32 s80, s74, s82\n"
"s_addc_u32 s81, s75, 0\n"
"s_mov_b32 m0, s82\n"
"s_barrier\n"
"global_load_lds_dwordx4 v2, s[80:81] sc1\n"
"global_load_lds_dwordx4 v2, s[80:81] offset:1024 sc1\n"
"s_cmp_lg_u32 s58, 3\n"
"s_cbranch_scc1 CH_md2\n"
"s_add_u32 s80, s74, 16384\n"
"s_addc_u32 s81, s75, 0\n"
"s_mov_b32 m0, 8192\n"
"s_nop 0\n"
"global_load_lds_dwordx4 v2, s[80:81] sc1\n"
"CH_md2:\n"
"s_add_u32 s82, s74, 17408\n"
"s_addc_u32 s83, s75, 0\n"
"v_mov_b32 v28, 0\n"
"global_load_dword v85, v28, s[82:83] sc1\n"
"ds_read_b128 v[88:91], v3 offset:0\n"
"ds_read_b128 v[92:95], v3 offset:1024\n"
"ds_read_b128 v[96:99], v3 offset:2048\n"
"ds_read_b128 v[100:103], v3 offset:3072\n"
"ds_read_b128 v[104:107], v3 offset:4096\n"
"ds_read_b128 v[108:111], v3 offset:5120\n"
"ds_read_b128 v[112:115], v3 offset:6144\n"
"ds_read_b128 v[116:119], v3 offset:7168\n"
"ds_read_b128 v[120:123], v3 offset:8192\n"
"ds_read_b128 v[124:127], v3 offset:9216\n"
"ds_read_b128 v[128:131], v3 offset:10240\n"
"ds_read_b128 v[132:135], v3 offset:11264\n"
"ds_read_b128 v[136:139], v3 offset:12288\n"
"ds_read_b128 v[140:143], v3 offset:13312\n"
"ds_read_b128 v[144:147], v3 offset:14336\n"
"ds_read_b128 v[148:151], v3 offset:15360\n"
"ds_read_b128 v[152:155], v3 offset:16384\n"
"ds_read_b128 v[156:159], v3 offset:17408\n"
"ds_read_b128 v[160:163], v3 offset:18432\n"
"ds_read_b128 v[164:167], v3 offset:19456\n"
"v_mov_b64 v[32:33], 0\n"
"v_mov_b64 v[34:35], 0\n"
"v_mov_b64 v[36:37], 0\n"
"v_mov_b64 v[38:39], 0\n"
"v_mov_b64 v[40:41], 0\n"
"v_mov_b64 v[42:43], 0\n"
"v_mov_b64 v[44:45], 0\n"
"v_mov_b64 v[46:47], 0\n"
"v_mov_b32 v28, 0x7f817f81\n"
"v_mov_b32 v29, 0x7f817f81\n"
"v_mov_b32 v30, 0x7f817f81\n"
"v_mov_b32 v31, 0x7f817f81\n"
"s_waitcnt lgkmcnt(0)\n"
"s_nop 0\n"
"s_branch CH_top\n"
"CH_nan_red:\n"
"s_add_u32 s89, s89, 1\n"
"s_and_b32 s80, s89, 63\n"
"s_cbranch_scc1 CH_red\n"
"s_mov_b64 s[80:81], 0\n"
"v_mov_b32 v28, 0x7f817f81\n"
"v_cmp_eq_u32 vcc, v28, v48\n"
"s_nop 4\n"
"s_or_b64 s[80:81], s[80:81], vcc\n"
"v_cmp_eq_u32 vcc, v28, v49\n"
"s_nop 4\n"
"s_or_b64 s[80:81], s[80:81], vcc\n"
"v_cmp_eq_u32 vcc, v28, v50\n"
"s_nop 4\n"
"s_or_b64 s[80:81], s[80:81], vcc\n"
"v_cmp_eq_u32 vcc, v28, v51\n"
"s_nop 4\n"
"s_or_b64 s[80:81], s[80:81], vcc\n"
"v_cmp_eq_u32 vcc, v28, v52\n"
"s_nop 4\n"
"s_or_b64 s[80:81], s[80:81], vcc\n"
"v_cmp_eq_u32 vcc, v28, v53\n"
"s_nop 4\n"
"s_or_b64 s[80:81], s[80:81], vcc\n"
"v_cmp_eq_u32 vcc, v28, v54\n"
"s_nop 4\n"
"s_or_b64 s[80:81], s[80:81], vcc\n"
"v_cmp_eq_u32 vcc, v28, v55\n"
"s_nop 4\n"
"s_or_b64 s[80:81], s[80:81], vcc\n"
"v_cmp_eq_u32 vcc, v28, v56\n"
"s_nop 4\n"
"s_or_b64 s[80:81], s[80:81], vcc\n"
"v_cmp_eq_u32 vcc, v28, v57\n"
"s_nop 4\n"
"s_or_b64 s[80:81], s[80:81], vcc\n"
"v_cmp_eq_u32 vcc, v28, v58\n"
"s_nop 4\n"
"s_or_b64 s[80:81], s[80:81], vcc\n"
"v_cmp_eq_u32 vcc, v28, v59\n"
"s_nop 4\n"
"s_or_b64 s[80:81], s[80:81], vcc\n"
"v_cmp_eq_u32 vcc, v28, v60\n"
"s_nop 4\n"
"s_or_b64 s[80:81], s[80:81], vcc\n"
"v_cmp_eq_u32 vcc, v28, v61\n"
"s_nop 4\n"
"s_or_b64 s[80:81], s[80:81], vcc\n"
"v_cmp_eq_u32 vcc, v28, v62\n"
"s_nop 4\n"
"s_or_b64 s[80:81], s[80:81], vcc\n"
"v_cmp_eq_u32 vcc, v28, v63\n"
"s_nop 4\n"
"s_or_b64 s[80:81], s[80:81], vcc\n"
"v_cmp_eq_u32 vcc, v28, v64\n"
"s_nop 4\n"
"s_or_b64 s[80:81], s[80:81], vcc\n"
"v_cmp_eq_u32 vcc, v28, v65\n"
"s_nop 4\n"
"s_or_b64 s[80:81], s[80:81], vcc\n"
"v_cmp_eq_u32 vcc, v28, v66\n"
"s_nop 4\n"
"s_or_b64 s[80:81], s[80:81], vcc\n"
"v_cmp_eq_u32 vcc, v28, v67\n"
"s_nop 4\n"
"s_or_b64 s[80:81], s[80:81], vcc\n"
"v_cmp_eq_u32 vcc, v28, v68\n"
"s_nop 4\n"
"s_or_b64 s[80:81], s[80:81], vcc\n"
"v_cmp_eq_u32 vcc, v28, v69\n"
"s_nop 4\n"
"s_or_b64 s[80:81], s[80:81], vcc\n"
"v_cmp_eq_u32 vcc, v28, v70\n"
"s_nop 4\n"
"s_or_b64 s[80:81], s[80:81], vcc\n"
"v_cmp_eq_u32 vcc, v28, v71\n"
"s_nop 4\n"
"s_or_b64 s[80:81], s[80:81], vcc\n"
"v_cmp_eq_u32 vcc, v28, v72\n"
"s_nop 4\n"
"s_or_b64 s[80:81], s[80:81], vcc\n"
"v_cmp_eq_u32 vcc, v28, v73\n"
"s_nop 4\n"
"s_or_b64 s[80:81], s[80:81], vcc\n"
"v_cmp_eq_u32 vcc, v28, v74\n"
"s_nop 4\n"
"s_or_b64 s[80:81], s[80:81], vcc\n"
"v_cmp_eq_u32 vcc, v28, v75\n"
"s_nop 4\n"
"s_or_b64 s[80:81], s[80:81], vcc\n"
"v_cmp_eq_u32 vcc, v28, v76\n"
"s_nop 4\n"
"s_or_b64 s[80:81], s[80:81], vcc\n"
"v_cmp_eq_u32 vcc, v28, v77\n"
"s_nop 4\n"
"s_or_b64 s[80:81], s[80:81], vcc\n"
"v_cmp_eq_u32 vcc, v28, v78\n"
"s_nop 4\n"
"s_or_b64 s[80:81], s[80:81], vcc\n"
"v_cmp_eq_u32 vcc, v28, v79\n"
"s_nop 4\n"
"s_or_b64 s[80:81], s[80:81], vcc\n"
"s_cmp_eq_u64 s[80:81], 0\n"
"s_cbranch_scc0 CH_red_again\n"
"s_mov_b64 exec, s[92:93]\n"
"global_store_dword v10, v82, s[76:77] sc1\n"
"s_mov_b64 exec, -1\n"
"s_branch CH_red_ret\n"
"CH_red_again:\n"
"s_cmp_lt_u32 s89, 0x40000\n"
"s_cbranch_scc1 CH_red\n"
"CH_abort:\n"
"s_add_u32 s80, s56, 82688\n"
"s_addc_u32 s81, s57, 0\n"
"v_mov_b32 v28, 0\n"
"v_mov_b32 v29, 1\n"
"global_store_dword v28, v29, s[80:81] sc1\n"
"s_branch CH_end\n"
"CH_last:\n"
"s_nop 15\n"
"v_mov_b32 v28, v164\n"
"v_mov_b32 v29, v165\n"
"v_mov_b32 v30, v166\n"
"v_mov_b32 v31, v167\n"
"v_permlane32_swap_b32 v28, v164\n"
"v_permlane32_swap_b32 v29, v165\n"
"v_permlane32_swap_b32 v30, v166\n"
"v_permlane32_swap_b32 v31, v167\n"
"v_add_f32 v28, v28, v164\n"
"v_add_f32 v29, v29, v165\n"
"v_add_f32 v30, v30, v166\n"
"v_add_f32 v31, v31, v167\n"
"v_cvt_pk_bf16_f32 v28, v28, v29\n"
"v_cvt_pk_bf16_f32 v29, v30, v31\n"
"s_mov_b32 exec_hi, 0\n"
"global_store_dwordx2 v12, v[28:29], s[54:55] sc1\n"
"s_mov_b64 exec, -1\n"
"s_cmp_lt_u32 s58, 2\n"
"s_cbranch_scc1 CH_nokm2\n"
"s_cmp_lg_u32 s61, 0\n"
"s_cbranch_scc1 CH_nokm2\n"
"ds_read_b64_tr_b16 v[48:49], v13 offset:0\n"
"ds_read_b64_tr_b16 v[50:51], v13 offset:128\n"
"ds_read_b64_tr_b16 v[52:53], v13 offset:512\n"
"ds_read_b64_tr_b16 v[54:55], v13 offset:640\n"
"s_waitcnt lgkmcnt(0)\n"
"s_mov_b64 exec, s[92:93]\n"
"global_store_dwordx4 v11, v[48:51], s[54:55] offset:0 sc1\n"
"global_store_dwordx4 v11, v[52:55], s[54:55] offset:512 sc1\n"
"s_mov_b64 exec, -1\n"
"s_nop 0\n"
"CH_nokm2:\n"
"s_waitcnt vmcnt(0)\n"
"v_mov_b32 v28, 0\n"
"v_mov_b32 v29, 1\n"
"s_mov_b64 exec, 1\n"
"global_atomic_add v28, v29, s[98:99] sc1\n"
"s_mov_b64 exec, -1\n"
"CH_end:\n"
"s_waitcnt vmcnt(0) lgkmcnt(0)\n"
"s_endpgm\n"
      : "+{v[0:15]}"(va), "+{v[16:23]}"(vb), "+{v[48:63]}"(vc), "+{v[64:79]}"(vd), "+{v84}"(ve), "+{s[40:41]}"(s0), "+{s[42:43]}"(s1), "+{s[44:45]}"(s2),
        "+{s[46:47]}"(wo), "+{s[48:49]}"(s4), "+{s[50:51]}"(win), "+{s[52:53]}"(kv), "+{s[54:55]}"(km), "+{s[56:57]}"(s8), "+{s58}"(t0), "+{s59}"(t1),
        "+{s60}"(t2), "+{s61}"(t3), "+{s62}"(t4), "+{s63}"(t5)
      :
      : "memory", "vcc", "scc", "v255", "a255", "s99");
  __builtin_unreachable();
}

DEVI void ld16_dev(u32x4& v, const char* base, unsigned voff) {
  asm volatile("global_load_dwordx4 %0, %1, %2 sc1" : "=v"(v) : "v"(voff), "s"(base) : "memory");
}
DEVI float swap_max(float x) { auto r = __builtin_amdgcn_permlane32_swap(__float_as_uint(x), __float_as_uint(x), false, false); return fmaxf(__uint_as_float(r[0]), __uint_as_float(r[1])); }
DEVI float swap_add(float x) { auto r = __builtin_amdgcn_permlane32_swap(__float_as_uint(x), __float_as_uint(x), false, false); return __uint_as_float(r[0]) + __uint_as_float(r[1]); }

DEVI void role_qa(const Params& p, int h, int w, int l) {
  constexpr unsigned XB = 0, QI = 73728, KT = 81920;
  u32x4 wq[36];
  const unsigned la = wlane256(w, l);
  XTile xt; xt.init(w, l);
#pragma unroll
  for (int t = 0; t < 3; t++) wstage256(p.awq, t, w, l, h * 256, 98304 + t * 16384);
  xt.fetch(p.ax, 0, w, XB);
#pragma unroll
  for (int t = 0; t < 9; t++) {
    if (t == 0) wait_vm<17>(); else if (t < 7) wait_vm<8>(); else if (t == 7) wait_vm<4>(); else wait_vm<0>();
    bar();
    if (t + 3 < 9) wstage256(p.awq, t + 3, w, l, h * 256, 98304 + ((t + 3) & 3) * 16384);
#pragma unroll
    for (int kk = 0; kk < 4; kk++) { wq[4 * t + kk] = wfrag256(98304 + (t & 3) * 16384, la, kk); asm volatile("" : "+a"(wq[4 * t + kk])); }
  }
  const unsigned hh = l >> 5, r = l & 31;
  const char* fkv = p.fl + FL_KV + h * 256;
  const char* fm = p.fl + FL_M + h * 256;
  char* tmo = p.fl + FL_TMO;
  const unsigned kro = ((r >> 4) * 256 + hh * 16 + (r & 15)) * 16;
  const unsigned vo = 8192 + ((2 * w + (r >> 4)) * 64 + hh * 16 + (r & 15)) * 16;
  const unsigned kmo = hh * 128 + (r & 7) * 16, vmo = 2048 + (32 * w + r) * 16 + 8 * hh;
  const float cs = 0.12751743f, masked = -1000000000.0f * 1.44269504f;
  unsigned kvn = 0, mn = 0;
#pragma unroll 1
  for (int s = 0; s < NSEG; s++) {
    const unsigned xb = XB + (s & 1) * 36864;
    const char* kv = p.wb + WS_KV + ((size_t)s * 32 + h) * 16384;
    const char* km = p.wb + WS_KM + ((size_t)s * 32 + h) * 4096;
    if (kvn < 4u * (s + 1)) kvn = wait_ge(fkv, 4u * (s + 1), tmo);
    u32x4 vf0, vf1;
    glds_dev(kv + w * 2048, l * 16, KT + w * 2048); glds_dev(kv + w * 2048 + 1024, l * 16, KT + w * 2048 + 1024);
    ld16_dev(vf0, kv, vo); ld16_dev(vf1, kv, vo + 512);
    xt.fetch(p.ax, s + 1 < NSEG ? s + 1 : s, w, XB + ((s + 1) & 1) * 36864);
    f32x16 aq = {};
    u32x4 xq[3] = {xt.frag(xb, 0), xt.frag(xb, 1), xt.frag(xb, 2)};
#pragma unroll
    for (int ks = 0; ks < 36; ks++) {
      const u32x4 xf = xq[ks % 3];
      if (ks + 3 < 36) xq[ks % 3] = xt.frag(xb, ks + 3);
      __builtin_amdgcn_sched_barrier(0);
      aq = mfma32(wq[ks], xf, aq);
    }
    *(LDSP u32x4*)(size_t)(QI + w * 2048 + l * 16) = u32x4{pk(aq[0], aq[1]), pk(aq[2], aq[3]), pk(aq[4], aq[5]), pk(aq[6], aq[7])};
    *(LDSP u32x4*)(size_t)(QI + w * 2048 + 1024 + l * 16) = u32x4{pk(aq[8], aq[9]), pk(aq[10], aq[11]), pk(aq[12], aq[13]), pk(aq[14], aq[15])};
    asm volatile("s_waitcnt vmcnt(9)" : "+v"(vf0), "+v"(vf1)::"memory");
    bar();
    u32x4 qf[8];
    f32x16 sx = {};
#pragma unroll
    for (int k = 0; k < 8; k++) { qf[k] = lds16(QI + k * 1024 + l * 16); sx = mfma32(lds16(KT + kro + k * 512), qf[k], sx); }
    float mx = -INFINITY;
#pragma unroll
    for (int i = 0; i < 16; i++) { const unsigned key = (i & 3) + 8 * (i >> 2) + 4 * hh; sx[i] = key <= r ? sx[i] * cs : masked; mx = fmaxf(mx, sx[i]); }
    if (mn < 8u * (s + 1)) mn = wait_ge(fm, 8u * (s + 1), tmo);
    u32x4 kmf[8]; u32x2 vmf;
#pragma unroll
    for (int k = 0; k < 8; k++) ld16_dev(kmf[k], km, kmo + k * 256);
    asm volatile("global_load_dwordx2 %0, %1, %2 sc1" : "=v"(vmf) : "v"(vmo), "s"(km) : "memory");
    asm volatile("s_waitcnt vmcnt(0)" : "+v"(kmf[0]), "+v"(kmf[1]), "+v"(kmf[2]), "+v"(kmf[3]), "+v"(kmf[4]), "+v"(kmf[5]), "+v"(kmf[6]), "+v"(kmf[7]), "+v"(vmf)::"memory");
    f32x16 sm = {};
#pragma unroll
    for (int k = 0; k < 8; k++) sm = mfma32(kmf[k], qf[k], sm);
#pragma unroll
    for (int i = 0; i < 4; i++) { sm[i] *= cs; mx = fmaxf(mx, sm[i]); }
    mx = swap_max(mx);
    float sum = 0.f;
#pragma unroll
    for (int i = 0; i < 16; i++) { sx[i] = __builtin_amdgcn_exp2f(sx[i] - mx); sum += sx[i]; }
#pragma unroll
    for (int i = 0; i < 4; i++) { sm[i] = __builtin_amdgcn_exp2f(sm[i] - mx); sum += sm[i]; }
    const float rs = __builtin_amdgcn_rcpf(swap_add(sum));
    f32x16 o = {};
    o = mfma32(vf0, u32x4{pk(sx[0], sx[1]), pk(sx[2], sx[3]), pk(sx[8], sx[9]), pk(sx[10], sx[11])}, o);
    o = mfma32(vf1, u32x4{pk(sx[4], sx[5]), pk(sx[6], sx[7]), pk(sx[12], sx[13]), pk(sx[14], sx[15])}, o);
    o = mfma32(u32x4{vmf[0], vmf[1], 0u, 0u}, u32x4{pk(sm[0], sm[1]), pk(sm[2], sm[3]), 0u, 0u}, o);
    char* ob = p.wb + WS_O + ((size_t)s * 32 + h) * 8192 + w * 2048;
    st16_dev(ob, l * 16, u32x4{pk(o[0] * rs, o[1] * rs), pk(o[2] * rs, o[3] * rs), pk(o[4] * rs, o[5] * rs), pk(o[6] * rs, o[7] * rs)});
    st16_dev(ob, l * 16 + 1024, u32x4{pk(o[8] * rs, o[9] * rs), pk(o[10] * rs, o[11] * rs), pk(o[12] * rs, o[13] * rs), pk(o[14] * rs, o[15] * rs)});
    wait_vm0();
    bar();
    if (w == 0 && l == 0) add_dev(p.fl + FL_O + (s * 4 + (h >> 3)) * 256);
  }
}

DEVI void role_out(const Params& p, int grp, int cg, int w, int l) {
  u32x4 wo[64];
  const unsigned g = l >> 4, q = (l >> 2) & 3, pp = l & 3, hh = l >> 5, r = l & 31;
  const unsigned sb = w * 32768, so = (l >> 2) * 1152 + cg * 64 + (l & 3) * 16;
  const unsigned ta = sb + (4 * (g >> 1) + q) * 64 + 32 * (g & 1) + 8 * pp;
  const char* wb = p.awo + (size_t)w * 1024 * 1152;
  auto stage = [&](int c) {
#pragma unroll
    for (int i = 0; i < 16; i++) glds(wb + (size_t)(c * 256 + i * 16) * 1152, so, sb + (c & 1) * 16384 + i * 1024);
  };
  stage(0); stage(1);
#pragma unroll
  for (int c = 0; c < 4; c++) {
    if (c < 3) wait_vm<16>(); else wait_vm<0>();
#pragma unroll
    for (int k = 0; k < 16; k++) {
      wo[16 * c + k] = cat(ldstr(ta + (c & 1) * 16384 + k * 1024), ldstr(ta + (c & 1) * 16384 + k * 1024 + 512));
      if (16 * c + k < 60) asm volatile("" : "+a"(wo[16 * c + k]));
    }
    wait_lgkm0();
    if (c + 2 < 4) stage(c + 2);
  }
  bar();
  char* tmo = p.fl + FL_TMO;
#pragma unroll 1
  for (int s = grp; s < NSEG; s += 2) {
    wait_ge(p.fl + FL_O + (s * 4 + w) * 256, 8u, tmo);
    const char* ob = p.wb + WS_O + ((size_t)s * 32 + 8 * w) * 8192;
    constexpr int W = 32;
    u32x4 of[W];
    f32x16 acc = {};
#pragma unroll
    for (int k = 0; k < W; k++) ld16_dev(of[k], ob + (k >> 2) * 4096, l * 16 + (k & 3) * 1024);
#pragma unroll
    for (int k = 0; k < 64; k++) {
      if (k < 64 - W) asm volatile("s_waitcnt vmcnt(%1)" : "+v"(of[k % W]) : "i"(W - 1) : "memory");
      else asm volatile("s_waitcnt vmcnt(%1)" : "+v"(of[k % W]) : "i"(63 - k) : "memory");
      acc = mfma32(wo[k], of[k % W], acc);
      __builtin_amdgcn_sched_barrier(0);
      if (k + W < 64) ld16_dev(of[k % W], ob + ((k + W) >> 2) * 4096, l * 16 + (k & 3) * 1024);
    }
    const unsigned rb = (s & 2) * 8192;
#pragma unroll
    for (int i = 0; i < 4; i++) *(LDSP f32x4*)(size_t)(rb + (w * 4 + i) * 1024 + l * 16) = f32x4{acc[4 * i], acc[4 * i + 1], acc[4 * i + 2], acc[4 * i + 3]};
    bar();
    f32x4 t = *(LDSP f32x4*)(size_t)(rb + w * 1024 + l * 16);
#pragma unroll
    for (int i = 1; i < 4; i++) t += *(LDSP f32x4*)(size_t)(rb + (i * 4 + w) * 1024 + l * 16);
    *(u32x2*)(p.out + (size_t)(32 * s + r) * 2304 + (32 * cg + 8 * w + 4 * hh) * 2) = u32x2{pk(t[0], t[1]), pk(t[2], t[3])};
  }
}

DEVI void gemm576(const char* W, const XTile& xt, unsigned act, unsigned wbuf, int w, int l, f32x16 (&acc)[5]) {
  unsigned vo[9];
#pragma unroll
  for (int i = 0; i < 9; i++) { const unsigned pos = (w * 9 + i) * 64 + l, row = pos / 72, c = pos - row * 72; vo[i] = row * 1152 + ((c ^ ((row & 2) << 1)) << 4); }
  const unsigned g = l >> 4, q = (l >> 2) & 3, pp = l & 3, T0 = w < 2 ? 5 * w : 4 * w + 2, be = q >> 1;
  const unsigned rowa = (8 * (g >> 1) + q) * 1152 + 32 * (g & 1) + 8 * pp;
  const unsigned te = rowa + (T0 ^ be) * 64, to = rowa + (((T0 + 1) ^ be) - 1) * 64;
  auto stage = [&](int k) {
#pragma unroll
    for (int i = 0; i < 9; i++) glds(W + (size_t)k * 36864, vo[i], wbuf + (k & 1) * 36864 + (w * 9 + i) * 1024);
  };
#pragma unroll
  for (int i = 0; i < 5; i++) acc[i] = f32x16{};
  stage(0);
#pragma unroll 2
  for (int k = 0; k < 18; k++) {
    wait_vm0(); bar();
    if (k + 1 < 18) stage(k + 1);
    const unsigned wb = wbuf + (k & 1) * 36864;
#pragma unroll
    for (int kk = 0; kk < 2; kk++) {
      const int ks = 2 * k + kk;
      const u32x4 b = lds16(act + xt.rd[2 * (k & 1) + kk] + (ks >> 2) * 128);
#pragma unroll
      for (int i = 0; i < 5; i++) {
        const unsigned a = wb + ((i & 1) ? to : te) + i * 64 + kk * 16 * 1152;
        acc[i] = mfma32(cat(ldstr(a), ldstr(a + 4 * 1152)), b, acc[i]);
      }
    }
  }
}
DEVI unsigned lds4(unsigned a) { return *(LDSP const unsigned*)(size_t)a; }
DEVI void role_b(const Params& p, int b, int w, int l) {
  constexpr unsigned XB = 0, WB = 36864, QB = 110592, K2 = 65536, VT = 73728, ONES = 147456, QS = 149504;
  const unsigned hh = l >> 5, r = l & 31, g = l >> 4, q = (l >> 2) & 3, pp = l & 3, t = w * 64 + l;
  XTile xt; xt.init(w, l);
  xt.fetch(p.bx, b, w, XB);
#pragma unroll
  for (int i = 0; i < 18; i++) *(LDSP u32x4*)(size_t)(WB + (i * 256 + t) * 16) = u32x4{};
  bar();
#pragma unroll 3
  for (int i = 0; i < 21; i++) {
    const unsigned n = i * 256 + t;
    if (n < 5184) { const unsigned row = n / 9, dw = n - row * 9;
      *(LDSP unsigned*)(size_t)(WB + row * 64 + dw * 4) = ((const unsigned*)p.bwk)[n];
      *(LDSP unsigned*)(size_t)(WB + 36864 + row * 64 + dw * 4) = ((const unsigned*)p.bwv)[n]; }
  }
  wait_vm0(); bar();
  char* img = p.wb + WS_BK;
  if (w < 2) {
    const unsigned col = w ? 16 * (g & 1) + 4 * pp : ((g & 1) ? 16 + 4 * pp : 8 * (pp & 1) + 4 * (pp >> 1));
    const unsigned ta = WB + w * 36864 + (8 * (g >> 1) + q) * 64 + col * 2;
    f32x16 a = {};
#pragma unroll 4
    for (int ks = 0; ks < 36; ks++) {
      const u32x4 wf = cat(ldstr(ta + ks * 1024), ldstr(ta + ks * 1024 + 256)), xf = lds16(XB + xt.rd[ks & 3] + (ks >> 2) * 128);
      a = w ? mfma32(xf, wf, a) : mfma32(wf, xf, a);
    }
    const u32x4 lo = {pk(a[0], a[1]), pk(a[2], a[3]), pk(a[4], a[5]), pk(a[6], a[7])};
    if (w == 0) {
      st16_dev(img + b * 1024, l * 16, lo);
      if (l < 32) { const unsigned v = pk(a[8], a[9]), o = l * 4; asm volatile("global_store_dword %0, %1, %2 sc1" ::"v"(o), "v"(v), "s"(img + K2 + b * 128) : "memory"); }
    } else if (r < 18) {
      st16_dev(img + VT + b * 1152, hh * 288 + r * 16, lo);
      st16_dev(img + VT + b * 1152, 576 + hh * 288 + r * 16, u32x4{pk(a[8], a[9]), pk(a[10], a[11]), pk(a[12], a[13]), pk(a[14], a[15])});
    }
    wait_vm0();
  }
  bar();
  if (t == 0) add_dev(p.fl + FL_B);
  f32x16 acc[5];
  const unsigned T0 = w < 2 ? 5 * w : 4 * w + 2;
  gemm576(p.bwq, xt, XB, WB, w, l, acc);
  const float cs = 0.34004648f;
#pragma unroll
  for (int i = 0; i < 5; i++)
    if (i < 4 || w < 2)
#pragma unroll
      for (int j = 0; j < 4; j++)
        *(LDSP u32x2*)(size_t)(QB + r * 1160 + ((T0 + i) * 32 + 8 * j + 4 * hh) * 2) =
            u32x2{pk(acc[i][4 * j] * cs, acc[i][4 * j + 1] * cs), pk(acc[i][4 * j + 2] * cs, acc[i][4 * j + 3] * cs)};
  bar();
  u32x4 q1[8]; unsigned q2r[8];
#pragma unroll
  for (int i = 0; i < 8; i++) {
    const unsigned a = QB + r * 1160 + (8 * w + i) * 36;
    q1[i] = u32x4{lds4(a + 16 * hh), lds4(a + 16 * hh + 4), lds4(a + 16 * hh + 8), lds4(a + 16 * hh + 12)};
    q2r[i] = hh ? 0u : lds4(a + 32);
  }
  if (w == 0) wait_ge(p.fl + FL_B, 64u, p.fl + FL_TMO);
  bar();
#pragma unroll 4
  for (int i = 0; i < 36; i++) glds_dev(img + (w * 36 + i) * 1024, l * 16, (w * 36 + i) * 1024);
  if (t < 4) *(LDSP u32x4*)(size_t)(ONES + (t >> 1) * 576 + (t & 1) * 16) = (t & 1) ? u32x4{} : u32x4{0x3F803F80u, 0x3F803F80u, 0x3F803F80u, 0x3F803F80u};
  wait_vm0(); bar();
  const unsigned vstep = r < 18 ? 1152u : 0u, v0a = r < 18 ? VT + hh * 288 + r * 16 : ONES + (r == 18 ? 0 : 16);
  unsigned op[8][5];
#pragma unroll
  for (int hp = 0; hp < 4; hp++) {
    u32x16 qq = {q1[2 * hp][0], q1[2 * hp][1], q1[2 * hp][2], q1[2 * hp][3], q1[2 * hp + 1][0], q1[2 * hp + 1][1], q1[2 * hp + 1][2], q1[2 * hp + 1][3],
                 q2r[2 * hp], 0u, 0u, 0u, q2r[2 * hp + 1], 0u, 0u, 0u};
    u32x4 ad = {l * 16u, K2 + r * 4, v0a, vstep};
    f32x16 oa, ob;
    asm volatile(
"s_mov_b32 s20, 0x41200000\n"
"v_mov_b32 v89, 0x3f803f80\n"
"v_mov_b32 v90, 0x3f80\n"
"v_mov_b32 v91, 0\n"
"v_mov_b32 v92, 0\n"
"v_mov_b32 v93, 0\n"
"v_mov_b64 v[96:97], 0\n"
"v_mov_b64 v[98:99], 0\n"
"v_mov_b64 v[100:101], 0\n"
"v_mov_b64 v[102:103], 0\n"
"v_mov_b64 v[104:105], 0\n"
"v_mov_b64 v[106:107], 0\n"
"v_mov_b64 v[108:109], 0\n"
"v_mov_b64 v[110:111], 0\n"
"v_mov_b64 v[112:113], 0\n"
"v_mov_b64 v[114:115], 0\n"
"v_mov_b64 v[116:117], 0\n"
"v_mov_b64 v[118:119], 0\n"
"v_mov_b64 v[120:121], 0\n"
"v_mov_b64 v[122:123], 0\n"
"v_mov_b64 v[124:125], 0\n"
"v_mov_b64 v[126:127], 0\n"
"ds_read_b128 v[84:87], v80\n"
"ds_read_b32 v88, v81\n"
"v_add_u32 v80, 0x400, v80\n"
"v_add_u32 v81, 0x80, v81\n"
"ds_read_b128 v[128:131], v82\n"
"ds_read_b128 v[132:135], v82 offset:576\n"
"v_add_u32 v82, v82, v83\n"
"s_waitcnt lgkmcnt(3)\n"
"v_mfma_f32_32x32x16_bf16 v[136:151], v[84:87], v[64:67], 0\n"
"v_mfma_f32_32x32x16_bf16 v[152:167], v[84:87], v[68:71], 0\n"
"s_waitcnt lgkmcnt(2)\n"
"v_mfma_f32_32x32x16_bf16 v[136:151], v[88:91], v[72:75], v[136:151]\n"
"v_mfma_f32_32x32x16_bf16 v[152:167], v[88:91], v[76:79], v[152:167]\n"
"ds_read_b128 v[84:87], v80\n"
"ds_read_b32 v88, v81\n"
"v_add_u32 v80, 0x400, v80\n"
"v_add_u32 v81, 0x80, v81\n"
"s_nop 0\n"
"v_max3_f32 v94, v136, v137, v138\n"
"v_max3_f32 v94, v94, v139, v140\n"
"v_max3_f32 v94, v94, v141, v142\n"
"v_max3_f32 v94, v94, v143, v144\n"
"v_max3_f32 v94, v94, v145, v146\n"
"v_max3_f32 v94, v94, v147, v148\n"
"v_max3_f32 v94, v94, v149, v150\n"
"v_max_f32 v94, v94, v151\n"
"v_max3_f32 v95, v152, v153, v154\n"
"v_max3_f32 v95, v95, v155, v156\n"
"v_max3_f32 v95, v95, v157, v158\n"
"v_max3_f32 v95, v95, v159, v160\n"
"v_max3_f32 v95, v95, v161, v162\n"
"v_max3_f32 v95, v95, v163, v164\n"
"v_max3_f32 v95, v95, v165, v166\n"
"v_max_f32 v95, v95, v167\n"
"v_mov_b32 v208, v94\n"
"s_nop 1\n"
"v_permlane32_swap_b32 v208, v94\n"
"v_max_f32 v208, v208, v94\n"
"v_add_f32 v209, v92, v208\n"
"v_sub_f32 v208, v209, v92\n"
"v_mov_b32 v92, v209\n"
"v_sub_f32 v136, v136, v208\n"
"v_sub_f32 v137, v137, v208\n"
"v_sub_f32 v138, v138, v208\n"
"v_sub_f32 v139, v139, v208\n"
"v_sub_f32 v140, v140, v208\n"
"v_sub_f32 v141, v141, v208\n"
"v_sub_f32 v142, v142, v208\n"
"v_sub_f32 v143, v143, v208\n"
"v_sub_f32 v144, v144, v208\n"
"v_sub_f32 v145, v145, v208\n"
"v_sub_f32 v146, v146, v208\n"
"v_sub_f32 v147, v147, v208\n"
"v_sub_f32 v148, v148, v208\n"
"v_sub_f32 v149, v149, v208\n"
"v_sub_f32 v150, v150, v208\n"
"v_sub_f32 v151, v151, v208\n"
"v_cvt_pk_bf16_f32 v211, -v209, -v209\n"
"v_and_b32 v213, 0xffff0000, v211\n"
"v_sub_f32 v209, -v209, v213\n"
"v_cvt_pk_bf16_f32 v212, v209, v209\n"
"v_and_b32 v213, 0xffff0000, v212\n"
"v_sub_f32 v209, v209, v213\n"
"v_and_b32 v211, 0xffff, v211\n"
"v_or_b32 v211, v211, v213\n"
"v_cvt_pk_bf16_f32 v212, v209, 0\n"
"s_mov_b32 exec_hi, 0\n"
"v_mov_b32 v73, v211\n"
"v_mov_b32 v74, v212\n"
"s_mov_b32 exec_hi, -1\n"
"v_mov_b32 v208, v95\n"
"s_nop 1\n"
"v_permlane32_swap_b32 v208, v95\n"
"v_max_f32 v208, v208, v95\n"
"v_add_f32 v209, v93, v208\n"
"v_sub_f32 v208, v209, v93\n"
"v_mov_b32 v93, v209\n"
"v_sub_f32 v152, v152, v208\n"
"v_sub_f32 v153, v153, v208\n"
"v_sub_f32 v154, v154, v208\n"
"v_sub_f32 v155, v155, v208\n"
"v_sub_f32 v156, v156, v208\n"
"v_sub_f32 v157, v157, v208\n"
"v_sub_f32 v158, v158, v208\n"
"v_sub_f32 v159, v159, v208\n"
"v_sub_f32 v160, v160, v208\n"
"v_sub_f32 v161, v161, v208\n"
"v_sub_f32 v162, v162, v208\n"
"v_sub_f32 v163, v163, v208\n"
"v_sub_f32 v164, v164, v208\n"
"v_sub_f32 v165, v165, v208\n"
"v_sub_f32 v166, v166, v208\n"
"v_sub_f32 v167, v167, v208\n"
"v_cvt_pk_bf16_f32 v211, -v209, -v209\n"
"v_and_b32 v213, 0xffff0000, v211\n"
"v_sub_f32 v209, -v209, v213\n"
"v_cvt_pk_bf16_f32 v212, v209, v209\n"
"v_and_b32 v213, 0xffff0000, v212\n"
"v_sub_f32 v209, v209, v213\n"
"v_and_b32 v211, 0xffff, v211\n"
"v_or_b32 v211, v211, v213\n"
"v_cvt_pk_bf16_f32 v212, v209, 0\n"
"s_mov_b32 exec_hi, 0\n"
"v_mov_b32 v77, v211\n"
"v_mov_b32 v78, v212\n"
"s_mov_b32 exec_hi, -1\n"
"s_mov_b32 s21, 31\n"
"s_waitcnt lgkmcnt(0)\n"
"BA%=_loop:\n"
"s_waitcnt lgkmcnt(3)\n"
"v_mfma_f32_32x32x16_bf16 v[168:183], v[84:87], v[64:67], 0\n"
"v_exp_f32 v136, v136\n"
"v_exp_f32 v137, v137\n"
"v_exp_f32 v138, v138\n"
"v_mfma_f32_32x32x16_bf16 v[184:199], v[84:87], v[68:71], 0\n"
"v_exp_f32 v139, v139\n"
"v_exp_f32 v140, v140\n"
"v_exp_f32 v141, v141\n"
"s_waitcnt lgkmcnt(2)\n"
"v_mfma_f32_32x32x16_bf16 v[168:183], v[88:91], v[72:75], v[168:183]\n"
"v_exp_f32 v142, v142\n"
"v_exp_f32 v143, v143\n"
"v_cvt_pk_bf16_f32 v200, v136, v137\n"
"v_cvt_pk_bf16_f32 v201, v138, v139\n"
"v_mfma_f32_32x32x16_bf16 v[184:199], v[88:91], v[76:79], v[184:199]\n"
"v_cvt_pk_bf16_f32 v202, v140, v141\n"
"v_cvt_pk_bf16_f32 v203, v142, v143\n"
"ds_read_b128 v[84:87], v80\n"
"ds_read_b32 v88, v81\n"
"v_add_u32 v80, 0x400, v80\n"
"v_add_u32 v81, 0x80, v81\n"
"v_exp_f32 v144, v144\n"
"s_waitcnt lgkmcnt(3)\n"
"v_mfma_f32_32x32x16_bf16 v[96:111], v[128:131], v[200:203], v[96:111]\n"
"v_exp_f32 v145, v145\n"
"v_exp_f32 v146, v146\n"
"v_exp_f32 v147, v147\n"
"v_exp_f32 v148, v148\n"
"v_exp_f32 v149, v149\n"
"v_exp_f32 v150, v150\n"
"v_exp_f32 v151, v151\n"
"v_cvt_pk_bf16_f32 v204, v144, v145\n"
"v_cvt_pk_bf16_f32 v205, v146, v147\n"
"v_cvt_pk_bf16_f32 v206, v148, v149\n"
"v_cvt_pk_bf16_f32 v207, v150, v151\n"
"v_exp_f32 v152, v152\n"
"s_waitcnt lgkmcnt(2)\n"
"v_mfma_f32_32x32x16_bf16 v[96:111], v[132:135], v[204:207], v[96:111]\n"
"v_exp_f32 v153, v153\n"
"v_exp_f32 v154, v154\n"
"v_exp_f32 v155, v155\n"
"v_exp_f32 v156, v156\n"
"v_exp_f32 v157, v157\n"
"v_exp_f32 v158, v158\n"
"v_exp_f32 v159, v159\n"
"v_cvt_pk_bf16_f32 v200, v152, v153\n"
"v_cvt_pk_bf16_f32 v201, v154, v155\n"
"v_cvt_pk_bf16_f32 v202, v156, v157\n"
"v_cvt_pk_bf16_f32 v203, v158, v159\n"
"v_max3_f32 v94, v168, v169, v170\n"
"v_max3_f32 v94, v94, v171, v172\n"
"v_max3_f32 v94, v94, v173, v174\n"
"v_max3_f32 v94, v94, v175, v176\n"
"v_mfma_f32_32x32x16_bf16 v[112:127], v[128:131], v[200:203], v[112:127]\n"
"v_max3_f32 v94, v94, v177, v178\n"
"v_max3_f32 v94, v94, v179, v180\n"
"v_max3_f32 v94, v94, v181, v182\n"
"v_max_f32 v94, v94, v183\n"
"v_max3_f32 v95, v184, v185, v186\n"
"v_max3_f32 v95, v95, v187, v188\n"
"v_max3_f32 v95, v95, v189, v190\n"
"v_max3_f32 v95, v95, v191, v192\n"
"v_max3_f32 v95, v95, v193, v194\n"
"v_max3_f32 v95, v95, v195, v196\n"
"v_max3_f32 v95, v95, v197, v198\n"
"v_max_f32 v95, v95, v199\n"
"v_max_f32 v208, v94, v95\n"
"v_cmp_lt_f32 vcc, s20, v208\n"
"v_exp_f32 v160, v160\n"
"v_exp_f32 v161, v161\n"
"v_exp_f32 v162, v162\n"
"v_exp_f32 v163, v163\n"
"v_exp_f32 v164, v164\n"
"v_exp_f32 v165, v165\n"
"v_exp_f32 v166, v166\n"
"v_exp_f32 v167, v167\n"
"v_cvt_pk_bf16_f32 v204, v160, v161\n"
"v_cvt_pk_bf16_f32 v205, v162, v163\n"
"v_cvt_pk_bf16_f32 v206, v164, v165\n"
"v_cvt_pk_bf16_f32 v207, v166, v167\n"
"s_nop 0\n"
"s_nop 0\n"
"v_mfma_f32_32x32x16_bf16 v[112:127], v[132:135], v[204:207], v[112:127]\n"
"ds_read_b128 v[128:131], v82\n"
"ds_read_b128 v[132:135], v82 offset:576\n"
"v_add_u32 v82, v82, v83\n"
"s_cbranch_vccnz BA%=_f0\n"
"BA%=_f0_r:\n"
"s_waitcnt lgkmcnt(3)\n"
"v_mfma_f32_32x32x16_bf16 v[136:151], v[84:87], v[64:67], 0\n"
"v_exp_f32 v168, v168\n"
"v_exp_f32 v169, v169\n"
"v_exp_f32 v170, v170\n"
"v_mfma_f32_32x32x16_bf16 v[152:167], v[84:87], v[68:71], 0\n"
"v_exp_f32 v171, v171\n"
"v_exp_f32 v172, v172\n"
"v_exp_f32 v173, v173\n"
"s_waitcnt lgkmcnt(2)\n"
"v_mfma_f32_32x32x16_bf16 v[136:151], v[88:91], v[72:75], v[136:151]\n"
"v_exp_f32 v174, v174\n"
"v_exp_f32 v175, v175\n"
"v_cvt_pk_bf16_f32 v200, v168, v169\n"
"v_cvt_pk_bf16_f32 v201, v170, v171\n"
"v_mfma_f32_32x32x16_bf16 v[152:167], v[88:91], v[76:79], v[152:167]\n"
"v_cvt_pk_bf16_f32 v202, v172, v173\n"
"v_cvt_pk_bf16_f32 v203, v174, v175\n"
"ds_read_b128 v[84:87], v80\n"
"ds_read_b32 v88, v81\n"
"v_add_u32 v80, 0x400, v80\n"
"v_add_u32 v81, 0x80, v81\n"
"v_exp_f32 v176, v176\n"
"s_waitcnt lgkmcnt(3)\n"
"v_mfma_f32_32x32x16_bf16 v[96:111], v[128:131], v[200:203], v[96:111]\n"
"v_exp_f32 v177, v177\n"
"v_exp_f32 v178, v178\n"
"v_exp_f32 v179, v179\n"
"v_exp_f32 v180, v180\n"
"v_exp_f32 v181, v181\n"
"v_exp_f32 v182, v182\n"
"v_exp_f32 v183, v183\n"
"v_cvt_pk_bf16_f32 v204, v176, v177\n"
"v_cvt_pk_bf16_f32 v205, v178, v179\n"
"v_cvt_pk_bf16_f32 v206, v180, v181\n"
"v_cvt_pk_bf16_f32 v207, v182, v183\n"
"v_exp_f32 v184, v184\n"
"s_waitcnt lgkmcnt(2)\n"
"v_mfma_f32_32x32x16_bf16 v[96:111], v[132:135], v[204:207], v[96:111]\n"
"v_exp_f32 v185, v185\n"
"v_exp_f32 v186, v186\n"
"v_exp_f32 v187, v187\n"
"v_exp_f32 v188, v188\n"
"v_exp_f32 v189, v189\n"
"v_exp_f32 v190, v190\n"
"v_exp_f32 v191, v191\n"
"v_cvt_pk_bf16_f32 v200, v184, v185\n"
"v_cvt_pk_bf16_f32 v201, v186, v187\n"
"v_cvt_pk_bf16_f32 v202, v188, v189\n"
"v_cvt_pk_bf16_f32 v203, v190, v191\n"
"v_max3_f32 v94, v136, v137, v138\n"
"v_max3_f32 v94, v94, v139, v140\n"
"v_max3_f32 v94, v94, v141, v142\n"
"v_max3_f32 v94, v94, v143, v144\n"
"v_mfma_f32_32x32x16_bf16 v[112:127], v[128:131], v[200:203], v[112:127]\n"
"v_max3_f32 v94, v94, v145, v146\n"
"v_max3_f32 v94, v94, v147, v148\n"
"v_max3_f32 v94, v94, v149, v150\n"
"v_max_f32 v94, v94, v151\n"
"v_max3_f32 v95, v152, v153, v154\n"
"v_max3_f32 v95, v95, v155, v156\n"
"v_max3_f32 v95, v95, v157, v158\n"
"v_max3_f32 v95, v95, v159, v160\n"
"v_max3_f32 v95, v95, v161, v162\n"
"v_max3_f32 v95, v95, v163, v164\n"
"v_max3_f32 v95, v95, v165, v166\n"
"v_max_f32 v95, v95, v167\n"
"v_max_f32 v208, v94, v95\n"
"v_cmp_lt_f32 vcc, s20, v208\n"
"v_exp_f32 v192, v192\n"
"v_exp_f32 v193, v193\n"
"v_exp_f32 v194, v194\n"
"v_exp_f32 v195, v195\n"
"v_exp_f32 v196, v196\n"
"v_exp_f32 v197, v197\n"
"v_exp_f32 v198, v198\n"
"v_exp_f32 v199, v199\n"
"v_cvt_pk_bf16_f32 v204, v192, v193\n"
"v_cvt_pk_bf16_f32 v205, v194, v195\n"
"v_cvt_pk_bf16_f32 v206, v196, v197\n"
"v_cvt_pk_bf16_f32 v207, v198, v199\n"
"s_nop 0\n"
"s_nop 0\n"
"v_mfma_f32_32x32x16_bf16 v[112:127], v[132:135], v[204:207], v[112:127]\n"
"ds_read_b128 v[128:131], v82\n"
"ds_read_b128 v[132:135], v82 offset:576\n"
"v_add_u32 v82, v82, v83\n"
"s_cbranch_vccnz BA%=_f1\n"
"BA%=_f1_r:\n"
"s_sub_u32 s21, s21, 1\n"
"s_cmp_lg_u32 s21, 0\n"
"s_cbranch_scc1 BA%=_loop\n"
"s_waitcnt lgkmcnt(3)\n"
"v_mfma_f32_32x32x16_bf16 v[168:183], v[84:87], v[64:67], 0\n"
"v_exp_f32 v136, v136\n"
"v_exp_f32 v137, v137\n"
"v_exp_f32 v138, v138\n"
"v_mfma_f32_32x32x16_bf16 v[184:199], v[84:87], v[68:71], 0\n"
"v_exp_f32 v139, v139\n"
"v_exp_f32 v140, v140\n"
"v_exp_f32 v141, v141\n"
"s_waitcnt lgkmcnt(2)\n"
"v_mfma_f32_32x32x16_bf16 v[168:183], v[88:91], v[72:75], v[168:183]\n"
"v_exp_f32 v142, v142\n"
"v_exp_f32 v143, v143\n"
"v_cvt_pk_bf16_f32 v200, v136, v137\n"
"v_cvt_pk_bf16_f32 v201, v138, v139\n"
"v_mfma_f32_32x32x16_bf16 v[184:199], v[88:91], v[76:79], v[184:199]\n"
"v_cvt_pk_bf16_f32 v202, v140, v141\n"
"v_cvt_pk_bf16_f32 v203, v142, v143\n"
"ds_read_b128 v[84:87], v80\n"
"ds_read_b32 v88, v81\n"
"v_add_u32 v80, 0x400, v80\n"
"v_add_u32 v81, 0x80, v81\n"
"v_exp_f32 v144, v144\n"
"s_waitcnt lgkmcnt(3)\n"
"v_mfma_f32_32x32x16_bf16 v[96:111], v[128:131], v[200:203], v[96:111]\n"
"v_exp_f32 v145, v145\n"
"v_exp_f32 v146, v146\n"
"v_exp_f32 v147, v147\n"
"v_exp_f32 v148, v148\n"
"v_exp_f32 v149, v149\n"
"v_exp_f32 v150, v150\n"
"v_exp_f32 v151, v151\n"
"v_cvt_pk_bf16_f32 v204, v144, v145\n"
"v_cvt_pk_bf16_f32 v205, v146, v147\n"
"v_cvt_pk_bf16_f32 v206, v148, v149\n"
"v_cvt_pk_bf16_f32 v207, v150, v151\n"
"v_exp_f32 v152, v152\n"
"s_waitcnt lgkmcnt(2)\n"
"v_mfma_f32_32x32x16_bf16 v[96:111], v[132:135], v[204:207], v[96:111]\n"
"v_exp_f32 v153, v153\n"
"v_exp_f32 v154, v154\n"
"v_exp_f32 v155, v155\n"
"v_exp_f32 v156, v156\n"
"v_exp_f32 v157, v157\n"
"v_exp_f32 v158, v158\n"
"v_exp_f32 v159, v159\n"
"v_cvt_pk_bf16_f32 v200, v152, v153\n"
"v_cvt_pk_bf16_f32 v201, v154, v155\n"
"v_cvt_pk_bf16_f32 v202, v156, v157\n"
"v_cvt_pk_bf16_f32 v203, v158, v159\n"
"v_max3_f32 v94, v168, v169, v170\n"
"v_max3_f32 v94, v94, v171, v172\n"
"v_max3_f32 v94, v94, v173, v174\n"
"v_max3_f32 v94, v94, v175, v176\n"
"v_mfma_f32_32x32x16_bf16 v[112:127], v[128:131], v[200:203], v[112:127]\n"
"v_max3_f32 v94, v94, v177, v178\n"
"v_max3_f32 v94, v94, v179, v180\n"
"v_max3_f32 v94, v94, v181, v182\n"
"v_max_f32 v94, v94, v183\n"
"v_max3_f32 v95, v184, v185, v186\n"
"v_max3_f32 v95, v95, v187, v188\n"
"v_max3_f32 v95, v95, v189, v190\n"
"v_max3_f32 v95, v95, v191, v192\n"
"v_max3_f32 v95, v95, v193, v194\n"
"v_max3_f32 v95, v95, v195, v196\n"
"v_max3_f32 v95, v95, v197, v198\n"
"v_max_f32 v95, v95, v199\n"
"v_max_f32 v208, v94, v95\n"
"v_cmp_lt_f32 vcc, s20, v208\n"
"v_exp_f32 v160, v160\n"
"v_exp_f32 v161, v161\n"
"v_exp_f32 v162, v162\n"
"v_exp_f32 v163, v163\n"
"v_exp_f32 v164, v164\n"
"v_exp_f32 v165, v165\n"
"v_exp_f32 v166, v166\n"
"v_exp_f32 v167, v167\n"
"v_cvt_pk_bf16_f32 v204, v160, v161\n"
"v_cvt_pk_bf16_f32 v205, v162, v163\n"
"v_cvt_pk_bf16_f32 v206, v164, v165\n"
"v_cvt_pk_bf16_f32 v207, v166, v167\n"
"s_nop 0\n"
"s_nop 0\n"
"v_mfma_f32_32x32x16_bf16 v[112:127], v[132:135], v[204:207], v[112:127]\n"
"ds_read_b128 v[128:131], v82\n"
"ds_read_b128 v[132:135], v82 offset:576\n"
"v_add_u32 v82, v82, v83\n"
"s_cbranch_vccnz BA%=_f2\n"
"BA%=_f2_r:\n"
"v_exp_f32 v168, v168\n"
"v_exp_f32 v169, v169\n"
"v_exp_f32 v170, v170\n"
"v_exp_f32 v171, v171\n"
"v_exp_f32 v172, v172\n"
"v_exp_f32 v173, v173\n"
"v_exp_f32 v174, v174\n"
"v_exp_f32 v175, v175\n"
"v_cvt_pk_bf16_f32 v200, v168, v169\n"
"v_cvt_pk_bf16_f32 v201, v170, v171\n"
"v_cvt_pk_bf16_f32 v202, v172, v173\n"
"v_cvt_pk_bf16_f32 v203, v174, v175\n"
"v_exp_f32 v176, v176\n"
"s_waitcnt lgkmcnt(1)\n"
"v_mfma_f32_32x32x16_bf16 v[96:111], v[128:131], v[200:203], v[96:111]\n"
"v_exp_f32 v177, v177\n"
"v_exp_f32 v178, v178\n"
"v_exp_f32 v179, v179\n"
"v_exp_f32 v180, v180\n"
"v_exp_f32 v181, v181\n"
"v_exp_f32 v182, v182\n"
"v_exp_f32 v183, v183\n"
"v_cvt_pk_bf16_f32 v204, v176, v177\n"
"v_cvt_pk_bf16_f32 v205, v178, v179\n"
"v_cvt_pk_bf16_f32 v206, v180, v181\n"
"v_cvt_pk_bf16_f32 v207, v182, v183\n"
"v_exp_f32 v184, v184\n"
"s_waitcnt lgkmcnt(0)\n"
"v_mfma_f32_32x32x16_bf16 v[96:111], v[132:135], v[204:207], v[96:111]\n"
"v_exp_f32 v185, v185\n"
"v_exp_f32 v186, v186\n"
"v_exp_f32 v187, v187\n"
"v_exp_f32 v188, v188\n"
"v_exp_f32 v189, v189\n"
"v_exp_f32 v190, v190\n"
"v_exp_f32 v191, v191\n"
"v_cvt_pk_bf16_f32 v200, v184, v185\n"
"v_cvt_pk_bf16_f32 v201, v186, v187\n"
"v_cvt_pk_bf16_f32 v202, v188, v189\n"
"v_cvt_pk_bf16_f32 v203, v190, v191\n"
"s_nop 1\n"
"v_mfma_f32_32x32x16_bf16 v[112:127], v[128:131], v[200:203], v[112:127]\n"
"v_exp_f32 v192, v192\n"
"v_exp_f32 v193, v193\n"
"v_exp_f32 v194, v194\n"
"v_exp_f32 v195, v195\n"
"v_exp_f32 v196, v196\n"
"v_exp_f32 v197, v197\n"
"v_exp_f32 v198, v198\n"
"v_exp_f32 v199, v199\n"
"v_cvt_pk_bf16_f32 v204, v192, v193\n"
"v_cvt_pk_bf16_f32 v205, v194, v195\n"
"v_cvt_pk_bf16_f32 v206, v196, v197\n"
"v_cvt_pk_bf16_f32 v207, v198, v199\n"
"s_nop 0\n"
"s_nop 0\n"
"v_mfma_f32_32x32x16_bf16 v[112:127], v[132:135], v[204:207], v[112:127]\n"
"s_waitcnt lgkmcnt(0)\n"
"s_nop 11\n"
"s_branch BA%=_end\n"
"BA%=_f0:\n"
"s_nop 15\n"
"v_mov_b32 v208, v94\n"
"s_nop 1\n"
"v_permlane32_swap_b32 v208, v94\n"
"v_max_f32 v208, v208, v94\n"
"v_max_f32 v208, 0, v208\n"
"v_add_f32 v209, v92, v208\n"
"v_sub_f32 v208, v209, v92\n"
"v_mov_b32 v92, v209\n"
"v_sub_f32 v168, v168, v208\n"
"v_sub_f32 v169, v169, v208\n"
"v_sub_f32 v170, v170, v208\n"
"v_sub_f32 v171, v171, v208\n"
"v_sub_f32 v172, v172, v208\n"
"v_sub_f32 v173, v173, v208\n"
"v_sub_f32 v174, v174, v208\n"
"v_sub_f32 v175, v175, v208\n"
"v_sub_f32 v176, v176, v208\n"
"v_sub_f32 v177, v177, v208\n"
"v_sub_f32 v178, v178, v208\n"
"v_sub_f32 v179, v179, v208\n"
"v_sub_f32 v180, v180, v208\n"
"v_sub_f32 v181, v181, v208\n"
"v_sub_f32 v182, v182, v208\n"
"v_sub_f32 v183, v183, v208\n"
"v_exp_f32 v210, -v208\n"
"s_nop 0\n"
"v_mul_f32 v96, v96, v210\n"
"v_mul_f32 v97, v97, v210\n"
"v_mul_f32 v98, v98, v210\n"
"v_mul_f32 v99, v99, v210\n"
"v_mul_f32 v100, v100, v210\n"
"v_mul_f32 v101, v101, v210\n"
"v_mul_f32 v102, v102, v210\n"
"v_mul_f32 v103, v103, v210\n"
"v_mul_f32 v104, v104, v210\n"
"v_mul_f32 v105, v105, v210\n"
"v_mul_f32 v106, v106, v210\n"
"v_mul_f32 v107, v107, v210\n"
"v_cvt_pk_bf16_f32 v211, -v209, -v209\n"
"v_and_b32 v213, 0xffff0000, v211\n"
"v_sub_f32 v209, -v209, v213\n"
"v_cvt_pk_bf16_f32 v212, v209, v209\n"
"v_and_b32 v213, 0xffff0000, v212\n"
"v_sub_f32 v209, v209, v213\n"
"v_and_b32 v211, 0xffff, v211\n"
"v_or_b32 v211, v211, v213\n"
"v_cvt_pk_bf16_f32 v212, v209, 0\n"
"s_mov_b32 exec_hi, 0\n"
"v_mov_b32 v73, v211\n"
"v_mov_b32 v74, v212\n"
"s_mov_b32 exec_hi, -1\n"
"v_mov_b32 v208, v95\n"
"s_nop 1\n"
"v_permlane32_swap_b32 v208, v95\n"
"v_max_f32 v208, v208, v95\n"
"v_max_f32 v208, 0, v208\n"
"v_add_f32 v209, v93, v208\n"
"v_sub_f32 v208, v209, v93\n"
"v_mov_b32 v93, v209\n"
"v_sub_f32 v184, v184, v208\n"
"v_sub_f32 v185, v185, v208\n"
"v_sub_f32 v186, v186, v208\n"
"v_sub_f32 v187, v187, v208\n"
"v_sub_f32 v188, v188, v208\n"
"v_sub_f32 v189, v189, v208\n"
"v_sub_f32 v190, v190, v208\n"
"v_sub_f32 v191, v191, v208\n"
"v_sub_f32 v192, v192, v208\n"
"v_sub_f32 v193, v193, v208\n"
"v_sub_f32 v194, v194, v208\n"
"v_sub_f32 v195, v195, v208\n"
"v_sub_f32 v196, v196, v208\n"
"v_sub_f32 v197, v197, v208\n"
"v_sub_f32 v198, v198, v208\n"
"v_sub_f32 v199, v199, v208\n"
"v_exp_f32 v210, -v208\n"
"s_nop 0\n"
"v_mul_f32 v112, v112, v210\n"
"v_mul_f32 v113, v113, v210\n"
"v_mul_f32 v114, v114, v210\n"
"v_mul_f32 v115, v115, v210\n"
"v_mul_f32 v116, v116, v210\n"
"v_mul_f32 v117, v117, v210\n"
"v_mul_f32 v118, v118, v210\n"
"v_mul_f32 v119, v119, v210\n"
"v_mul_f32 v120, v120, v210\n"
"v_mul_f32 v121, v121, v210\n"
"v_mul_f32 v122, v122, v210\n"
"v_mul_f32 v123, v123, v210\n"
"v_cvt_pk_bf16_f32 v211, -v209, -v209\n"
"v_and_b32 v213, 0xffff0000, v211\n"
"v_sub_f32 v209, -v209, v213\n"
"v_cvt_pk_bf16_f32 v212, v209, v209\n"
"v_and_b32 v213, 0xffff0000, v212\n"
"v_sub_f32 v209, v209, v213\n"
"v_and_b32 v211, 0xffff, v211\n"
"v_or_b32 v211, v211, v213\n"
"v_cvt_pk_bf16_f32 v212, v209, 0\n"
"s_mov_b32 exec_hi, 0\n"
"v_mov_b32 v77, v211\n"
"v_mov_b32 v78, v212\n"
"s_mov_b32 exec_hi, -1\n"
"s_nop 0\n"
"s_branch BA%=_f0_r\n"
"BA%=_f1:\n"
"s_nop 15\n"
"v_mov_b32 v208, v94\n"
"s_nop 1\n"
"v_permlane32_swap_b32 v208, v94\n"
"v_max_f32 v208, v208, v94\n"
"v_max_f32 v208, 0, v208\n"
"v_add_f32 v209, v92, v208\n"
"v_sub_f32 v208, v209, v92\n"
"v_mov_b32 v92, v209\n"
"v_sub_f32 v136, v136, v208\n"
"v_sub_f32 v137, v137, v208\n"
"v_sub_f32 v138, v138, v208\n"
"v_sub_f32 v139, v139, v208\n"
"v_sub_f32 v140, v140, v208\n"
"v_sub_f32 v141, v141, v208\n"
"v_sub_f32 v142, v142, v208\n"
"v_sub_f32 v143, v143, v208\n"
"v_sub_f32 v144, v144, v208\n"
"v_sub_f32 v145, v145, v208\n"
"v_sub_f32 v146, v146, v208\n"
"v_sub_f32 v147, v147, v208\n"
"v_sub_f32 v148, v148, v208\n"
"v_sub_f32 v149, v149, v208\n"
"v_sub_f32 v150, v150, v208\n"
"v_sub_f32 v151, v151, v208\n"
"v_exp_f32 v210, -v208\n"
"s_nop 0\n"
"v_mul_f32 v96, v96, v210\n"
"v_mul_f32 v97, v97, v210\n"
"v_mul_f32 v98, v98, v210\n"
"v_mul_f32 v99, v99, v210\n"
"v_mul_f32 v100, v100, v210\n"
"v_mul_f32 v101, v101, v210\n"
"v_mul_f32 v102, v102, v210\n"
"v_mul_f32 v103, v103, v210\n"
"v_mul_f32 v104, v104, v210\n"
"v_mul_f32 v105, v105, v210\n"
"v_mul_f32 v106, v106, v210\n"
"v_mul_f32 v107, v107, v210\n"
"v_cvt_pk_bf16_f32 v211, -v209, -v209\n"
"v_and_b32 v213, 0xffff0000, v211\n"
"v_sub_f32 v209, -v209, v213\n"
"v_cvt_pk_bf16_f32 v212, v209, v209\n"
"v_and_b32 v213, 0xffff0000, v212\n"
"v_sub_f32 v209, v209, v213\n"
"v_and_b32 v211, 0xffff, v211\n"
"v_or_b32 v211, v211, v213\n"
"v_cvt_pk_bf16_f32 v212, v209, 0\n"
"s_mov_b32 exec_hi, 0\n"
"v_mov_b32 v73, v211\n"
"v_mov_b32 v74, v212\n"
"s_mov_b32 exec_hi, -1\n"
"v_mov_b32 v208, v95\n"
"s_nop 1\n"
"v_permlane32_swap_b32 v208, v95\n"
"v_max_f32 v208, v208, v95\n"
"v_max_f32 v208, 0, v208\n"
"v_add_f32 v209, v93, v208\n"
"v_sub_f32 v208, v209, v93\n"
"v_mov_b32 v93, v209\n"
"v_sub_f32 v152, v152, v208\n"
"v_sub_f32 v153, v153, v208\n"
"v_sub_f32 v154, v154, v208\n"
"v_sub_f32 v155, v155, v208\n"
"v_sub_f32 v156, v156, v208\n"
"v_sub_f32 v157, v157, v208\n"
"v_sub_f32 v158, v158, v208\n"
"v_sub_f32 v159, v159, v208\n"
"v_sub_f32 v160, v160, v208\n"
"v_sub_f32 v161, v161, v208\n"
"v_sub_f32 v162, v162, v208\n"
"v_sub_f32 v163, v163, v208\n"
"v_sub_f32 v164, v164, v208\n"
"v_sub_f32 v165, v165, v208\n"
"v_sub_f32 v166, v166, v208\n"
"v_sub_f32 v167, v167, v208\n"
"v_exp_f32 v210, -v208\n"
"s_nop 0\n"
"v_mul_f32 v112, v112, v210\n"
"v_mul_f32 v113, v113, v210\n"
"v_mul_f32 v114, v114, v210\n"
"v_mul_f32 v115, v115, v210\n"
"v_mul_f32 v116, v116, v210\n"
"v_mul_f32 v117, v117, v210\n"
"v_mul_f32 v118, v118, v210\n"
"v_mul_f32 v119, v119, v210\n"
"v_mul_f32 v120, v120, v210\n"
"v_mul_f32 v121, v121, v210\n"
"v_mul_f32 v122, v122, v210\n"
"v_mul_f32 v123, v123, v210\n"
"v_cvt_pk_bf16_f32 v211, -v209, -v209\n"
"v_and_b32 v213, 0xffff0000, v211\n"
"v_sub_f32 v209, -v209, v213\n"
"v_cvt_pk_bf16_f32 v212, v209, v209\n"
"v_and_b32 v213, 0xffff0000, v212\n"
"v_sub_f32 v209, v209, v213\n"
"v_and_b32 v211, 0xffff, v211\n"
"v_or_b32 v211, v211, v213\n"
"v_cvt_pk_bf16_f32 v212, v209, 0\n"
"s_mov_b32 exec_hi, 0\n"
"v_mov_b32 v77, v211\n"
"v_mov_b32 v78, v212\n"
"s_mov_b32 exec_hi, -1\n"
"s_nop 0\n"
"s_branch BA%=_f1_r\n"
"BA%=_f2:\n"
"s_nop 15\n"
"v_mov_b32 v208, v94\n"
"s_nop 1\n"
"v_permlane32_swap_b32 v208, v94\n"
"v_max_f32 v208, v208, v94\n"
"v_max_f32 v208, 0, v208\n"
"v_add_f32 v209, v92, v208\n"
"v_sub_f32 v208, v209, v92\n"
"v_mov_b32 v92, v209\n"
"v_sub_f32 v168, v168, v208\n"
"v_sub_f32 v169, v169, v208\n"
"v_sub_f32 v170, v170, v208\n"
"v_sub_f32 v171, v171, v208\n"
"v_sub_f32 v172, v172, v208\n"
"v_sub_f32 v173, v173, v208\n"
"v_sub_f32 v174, v174, v208\n"
"v_sub_f32 v175, v175, v208\n"
"v_sub_f32 v176, v176, v208\n"
"v_sub_f32 v177, v177, v208\n"
"v_sub_f32 v178, v178, v208\n"
"v_sub_f32 v179, v179, v208\n"
"v_sub_f32 v180, v180, v208\n"
"v_sub_f32 v181, v181, v208\n"
"v_sub_f32 v182, v182, v208\n"
"v_sub_f32 v183, v183, v208\n"
"v_exp_f32 v210, -v208\n"
"s_nop 0\n"
"v_mul_f32 v96, v96, v210\n"
"v_mul_f32 v97, v97, v210\n"
"v_mul_f32 v98, v98, v210\n"
"v_mul_f32 v99, v99, v210\n"
"v_mul_f32 v100, v100, v210\n"
"v_mul_f32 v101, v101, v210\n"
"v_mul_f32 v102, v102, v210\n"
"v_mul_f32 v103, v103, v210\n"
"v_mul_f32 v104, v104, v210\n"
"v_mul_f32 v105, v105, v210\n"
"v_mul_f32 v106, v106, v210\n"
"v_mul_f32 v107, v107, v210\n"
"v_cvt_pk_bf16_f32 v211, -v209, -v209\n"
"v_and_b32 v213, 0xffff0000, v211\n"
"v_sub_f32 v209, -v209, v213\n"
"v_cvt_pk_bf16_f32 v212, v209, v209\n"
"v_and_b32 v213, 0xffff0000, v212\n"
"v_sub_f32 v209, v209, v213\n"
"v_and_b32 v211, 0xffff, v211\n"
"v_or_b32 v211, v211, v213\n"
"v_cvt_pk_bf16_f32 v212, v209, 0\n"
"s_mov_b32 exec_hi, 0\n"
"v_mov_b32 v73, v211\n"
"v_mov_b32 v74, v212\n"
"s_mov_b32 exec_hi, -1\n"
"v_mov_b32 v208, v95\n"
"s_nop 1\n"
"v_permlane32_swap_b32 v208, v95\n"
"v_max_f32 v208, v208, v95\n"
"v_max_f32 v208, 0, v208\n"
"v_add_f32 v209, v93, v208\n"
"v_sub_f32 v208, v209, v93\n"
"v_mov_b32 v93, v209\n"
"v_sub_f32 v184, v184, v208\n"
"v_sub_f32 v185, v185, v208\n"
"v_sub_f32 v186, v186, v208\n"
"v_sub_f32 v187, v187, v208\n"
"v_sub_f32 v188, v188, v208\n"
"v_sub_f32 v189, v189, v208\n"
"v_sub_f32 v190, v190, v208\n"
"v_sub_f32 v191, v191, v208\n"
"v_sub_f32 v192, v192, v208\n"
"v_sub_f32 v193, v193, v208\n"
"v_sub_f32 v194, v194, v208\n"
"v_sub_f32 v195, v195, v208\n"
"v_sub_f32 v196, v196, v208\n"
"v_sub_f32 v197, v197, v208\n"
"v_sub_f32 v198, v198, v208\n"
"v_sub_f32 v199, v199, v208\n"
"v_exp_f32 v210, -v208\n"
"s_nop 0\n"
"v_mul_f32 v112, v112, v210\n"
"v_mul_f32 v113, v113, v210\n"
"v_mul_f32 v114, v114, v210\n"
"v_mul_f32 v115, v115, v210\n"
"v_mul_f32 v116, v116, v210\n"
"v_mul_f32 v117, v117, v210\n"
"v_mul_f32 v118, v118, v210\n"
"v_mul_f32 v119, v119, v210\n"
"v_mul_f32 v120, v120, v210\n"
"v_mul_f32 v121, v121, v210\n"
"v_mul_f32 v122, v122, v210\n"
"v_mul_f32 v123, v123, v210\n"
"v_cvt_pk_bf16_f32 v211, -v209, -v209\n"
"v_and_b32 v213, 0xffff0000, v211\n"
"v_sub_f32 v209, -v209, v213\n"
"v_cvt_pk_bf16_f32 v212, v209, v209\n"
"v_and_b32 v213, 0xffff0000, v212\n"
"v_sub_f32 v209, v209, v213\n"
"v_and_b32 v211, 0xffff, v211\n"
"v_or_b32 v211, v211, v213\n"
"v_cvt_pk_bf16_f32 v212, v209, 0\n"
"s_mov_b32 exec_hi, 0\n"
"v_mov_b32 v77, v211\n"
"v_mov_b32 v78, v212\n"
"s_mov_b32 exec_hi, -1\n"
"s_nop 0\n"
"s_branch BA%=_f2_r\n"
"BA%=_end:\n"
        : "+{v[64:79]}"(qq), "+{v[80:83]}"(ad), "={v[96:111]}"(oa), "={v[112:127]}"(ob) : : "memory", "vcc", "scc", "v84", "v85", "v86", "v87", "v88", "v89", "v90", "v91", "v92", "v93", "v94", "v95", "v128", "v129", "v130", "v131", "v132", "v133", "v134", "v135", "v136", "v137", "v138", "v139", "v140", "v141", "v142", "v143", "v144", "v145", "v146", "v147", "v148", "v149", "v150", "v151", "v152", "v153", "v154", "v155", "v156", "v157", "v158", "v159", "v160", "v161", "v162", "v163", "v164", "v165", "v166", "v167", "v168", "v169", "v170", "v171", "v172", "v173", "v174", "v175", "v176", "v177", "v178", "v179", "v180", "v181", "v182", "v183", "v184", "v185", "v186", "v187", "v188", "v189", "v190", "v191", "v192", "v193", "v194", "v195", "v196", "v197", "v198", "v199", "v200", "v201", "v202", "v203", "v204", "v205", "v206", "v207", "v208", "v209", "v210", "v211", "v212", "v213", "v214", "v215", "s20", "s21");
#pragma unroll
    for (int u = 0; u < 2; u++) {
      const f32x16& o = u ? ob : oa;
      const auto x = __builtin_amdgcn_permlane32_swap(__float_as_uint(o[10]), __float_as_uint(o[10]), false, false);
      const float rl = __builtin_amdgcn_rcpf(__uint_as_float(x[0]));
#pragma unroll
      for (int j = 0; j < 5; j++) op[2 * hp + u][j] = pk(o[2 * j] * rl, o[2 * j + 1] * rl);
    }
  }
  bar();
#pragma unroll
  for (int i = 0; i < 8; i++)
#pragma unroll
    for (int j = 0; j < 5; j++) {
      const unsigned col = (8 * w + i) * 18 + (j < 4 ? 8 * (j >> 1) + 4 * hh + 2 * (j & 1) : 16);
      if (j < 4 || hh == 0) *(LDSP unsigned*)(size_t)(XB + r * 1152 + (((col >> 3) ^ ((r >> 1) & 7)) << 4) + (col & 7) * 2) = op[i][j];
    }
  gemm576(p.bwo, xt, XB, WB, w, l, acc);
#pragma unroll
  for (int i = 0; i < 5; i++)
    if (i < 4 || w < 2)
#pragma unroll
      for (int j = 0; j < 4; j++)
        *(u32x2*)(p.out + (size_t)(32 * b + r) * 2304 + 1152 + ((T0 + i) * 32 + 8 * j + 4 * hh) * 2) =
            u32x2{pk(acc[i][4 * j], acc[i][4 * j + 1]), pk(acc[i][4 * j + 2], acc[i][4 * j + 3])};
}

__global__ __launch_bounds__(256, 1) void mega(Params p) {
  const int l = threadIdx.x & 63, w = __builtin_amdgcn_readfirstlane(threadIdx.x >> 6);
  const int x = blockIdx.x & 7, y = blockIdx.x >> 3;
  if (x < 4) {
    if (y < 16) role_chain(p, x * 16 + y, w, l);
    else role_b(p, x * 16 + y - 16, w, l);
  } else if (y < 8) role_kv(p, (x - 4) * 8 + y, w, l);
  else if (y < 16) role_qa(p, (x - 4) * 8 + y - 8, w, l);
  else if (y < 25) role_out(p, (x - 4) >> 1, (x & 1) * 9 + y - 16, w, l);
}

extern "C" void kernel_launch(void* const* d_in, const int* in_sizes, int n_in, void* d_out, int out_size, void* d_ws, size_t ws_size,
                              hipStream_t stream) {
  Params p;
  p.ax = (const char*)d_in[0]; p.amem = (const char*)d_in[1]; p.awq = (const char*)d_in[2]; p.awk = (const char*)d_in[3];
  p.awv = (const char*)d_in[4]; p.awo = (const char*)d_in[5]; p.bx = (const char*)d_in[7]; p.bwq = (const char*)d_in[8];
  p.bwk = (const char*)d_in[9]; p.bwv = (const char*)d_in[10]; p.bwo = (const char*)d_in[11];
  p.out = (char*)d_out; p.fl = (char*)d_ws;
  p.wb = (char*)(((size_t)d_ws + FL_BYTES + (2u << 20) - 1) & ~(size_t)((2u << 20) - 1));
  (void)hipFuncSetAttribute((const void*)mega, hipFuncAttributeMaxDynamicSharedMemorySize, 160 * 1024);
  (void)hipMemsetAsync(d_ws, 0, FL_BYTES, stream);
  mega<<<256, 256, 160 * 1024, stream>>>(p);
}
```
